```python
import torch

def _softmax(x, axis=-1):
    return torch.softmax(x, dim=axis)
def _gelu(x):
    return torch.nn.functional.gelu(x, approximate="tanh")
def _silu(x):
    return torch.nn.functional.silu(x)
def _sigmoid(x):
    return torch.sigmoid(x)
def _layernorm(x, axis=-1, eps=1e-5):
    m = x.mean(dim=axis, keepdim=True)
    v = x.var(dim=axis, keepdim=True, unbiased=False)
    return (x - m) / torch.sqrt(v + eps)
def _scatter_add(tgt, idx, src, axis=0):
    return tgt.clone().index_add_(axis, idx, src)
def _segment_sum(src, seg, n):
    out = torch.zeros((n,) + src.shape[1:], dtype=src.dtype)
    return out.index_add_(0, seg, src)
def _pad(x, pw):
    flat = tuple(v for pair in reversed(pw) for v in pair)
    return torch.nn.functional.pad(x, flat)

kernel_name = 'interleave_layers__sliding_block_cached__glu_gated_swiglu__decode'

def setup_inputs():

    _w = (lambda a: (lambda t: t.to(torch.bfloat16) if t.is_floating_point() else t)(torch.as_tensor(a)))
    def _A_setup():
        B, Sq, D = 1, 1, 448
        Hq, Hkv, Sk, Dh = 16, 8, 8192, 256
        assert Hq % Hkv == 0, (Hq, Hkv)
        return {
            "x": _w((lambda *s: torch.randn(*s, dtype=torch.bfloat16) * 0.05)(B, Sq, D)),
            "k_cache": _w((lambda *s: torch.randn(*s, dtype=torch.bfloat16) * 1.6)(B, Hkv, Sk, Dh)),
            "v_cache": _w((lambda *s: torch.randn(*s, dtype=torch.bfloat16) * 0.8)(B, Hkv, Sk, Dh)),
            "wq": _w((lambda *s: torch.randn(*s, dtype=torch.bfloat16) * 1.6)(D, Hq * Dh) * (D ** -0.5)),
            "wo": _w((lambda *s: torch.randn(*s, dtype=torch.bfloat16) * 0.8)(Hq * Dh, D) * ((Hq * Dh) ** -0.5)),
        }
    _A_in = _A_setup()

    def _B_setup():
        B, S, D, F = 1, 1, 448, 1792
        return {
            "x": _w((lambda *s: torch.randn(*s, dtype=torch.bfloat16) * 0.05)(B, S, D)),
            "w1": _w((lambda *s: torch.randn(*s, dtype=torch.bfloat16) * 0.4)(D, F) * (2.0 / D) ** 0.5),
            "w3": _w((lambda *s: torch.randn(*s, dtype=torch.bfloat16) * 0.4)(D, F) * (2.0 / D) ** 0.5),
            "w2": _w((lambda *s: torch.randn(*s, dtype=torch.bfloat16) * 0.4)(F, D) * (2.0 / F) ** 0.5),
        }
    _B_in = _B_setup()

    return {
        "A_x": _A_in["x"],
        "A_k_cache": _A_in["k_cache"],
        "A_v_cache": _A_in["v_cache"],
        "A_wq": _A_in["wq"],
        "A_wo": _A_in["wo"],
        "B_x": _B_in["x"],
        "B_w1": _B_in["w1"],
        "B_w3": _B_in["w3"],
        "B_w2": _B_in["w2"],
    }

def reference(A_x, A_k_cache, A_v_cache, A_wq, A_wo, B_x, B_w1, B_w3, B_w2):
    def _A_ref(x, k_cache, v_cache, wq, wo):
        B, Sq, D = x.shape
        Hq, Dh = 16, 256
        Hkv, Sk = k_cache.shape[1], k_cache.shape[2]
        W = min(4096, max(256, Sk // 4))
        rep = Hq // Hkv
        q = (lambda x, *d: x.permute(*d))((x @ wq).reshape(B, Sq, Hq, Dh), 0, 2, 1, 3)
        k = (lambda x, n, axis: torch.repeat_interleave(x, n, dim=axis))(k_cache[:, :, Sk - W:, :], rep, axis=1)
        v = (lambda x, n, axis: torch.repeat_interleave(x, n, dim=axis))(v_cache[:, :, Sk - W:, :], rep, axis=1)
        scores = torch.einsum("bhqd,bhkd->bhqk", q, k) * (Dh ** -0.5)
        p = _softmax(scores, axis=-1)
        o = torch.einsum("bhqk,bhkd->bhqd", p, v)
        return (lambda x, *d: x.permute(*d))(o, 0, 2, 1, 3).reshape(B, Sq, Hq * Dh) @ wo

    def _B_ref(x, w1, w3, w2):
        gate = torch.einsum("bsd,df->bsf", x, w1)
        up = torch.einsum("bsd,df->bsf", x, w3)
        h = _silu(gate) * up
        out = torch.einsum("bsf,fd->bsd", h, w2)
        return out

    x = A_x
    x = x + _A_ref(_layernorm(x, axis=-1), A_k_cache, A_v_cache, A_wq, A_wo)
    x = x + _B_ref(_layernorm(x, axis=-1), B_w1, B_w3, B_w2)
    x = x + _A_ref(_layernorm(x, axis=-1), A_k_cache, A_v_cache, A_wq, A_wo)
    x = x + _B_ref(_layernorm(x, axis=-1), B_w1, B_w3, B_w2)
    return x
```

```cpp
#include <hip/hip_runtime.h>
#include <hip/hip_bf16.h>
#include <stdint.h>

#define MIMIC 1

constexpr int D = 448, HQ = 16, HKV = 8, SK = 8192, DH = 256, W = 2048, F = 1792;
typedef unsigned short u16;

__device__ __forceinline__ float bf2f(u16 b) { return __uint_as_float(((unsigned)b) << 16); }
__device__ __forceinline__ float rbf(float f) {
  unsigned u = __float_as_uint(f); u += 0x7FFFu + ((u >> 16) & 1u); return __uint_as_float(u & 0xFFFF0000u); }
__device__ __forceinline__ float mr(float f) { return MIMIC ? rbf(f) : f; }

__device__ float block_sum(float v, float* sm) {
  for (int o = 32; o > 0; o >>= 1) v += __shfl_xor(v, o);
  __syncthreads();
  if ((threadIdx.x & 63) == 0) sm[threadIdx.x >> 6] = v;
  __syncthreads();
  float s = 0; for (int i = 0; i < (int)(blockDim.x >> 6); ++i) s += sm[i];
  return s;
}
__device__ void ln_to_lds(const float* x, float* xn, float* sm) {
  int t = threadIdx.x;
  float a = 0; for (int i = t; i < D; i += blockDim.x) a += x[i];
  float m = mr(block_sum(a, sm) / D);
  float b = 0;
  float a2 = 0; for (int i = t; i < D; i += blockDim.x) a2 += x[i];
  float mf = block_sum(a2, sm) / D;
  b = 0; for (int i = t; i < D; i += blockDim.x) { float d = x[i] - mf; b += d * d; }
  float v = mr(block_sum(b, sm) / D);
  float den = mr(sqrtf(mr(v + 1e-5f)));
  for (int i = t; i < D; i += blockDim.x) xn[i] = rbf(mr(x[i] - m) / den);
  __syncthreads();
}

__global__ void k_init(const u16* xin, float* x) { int i = threadIdx.x + blockIdx.x * blockDim.x; if (i < D) x[i] = bf2f(xin[i]); }

__global__ void k_ln_gemv(const float* x, const u16* w, int N, float* y) {
  __shared__ float xn[D]; __shared__ float sm[16]; __shared__ float red[4][64];
  ln_to_lds(x, xn, sm);
  int lane = threadIdx.x & 63, wv = threadIdx.x >> 6, col = blockIdx.x * 64 + lane;
  float acc = 0;
  for (int k = wv * 112; k < wv * 112 + 112; ++k) acc += xn[k] * bf2f(w[(size_t)k * N + col]);
  red[wv][lane] = acc; __syncthreads();
  if (wv == 0) y[col] = rbf(red[0][lane] + red[1][lane] + red[2][lane] + red[3][lane]);
}
__global__ void k_attn(const float* q, const u16* kc, const u16* vc, float* po, float* pml) {
  __shared__ float qs[2][DH]; __shared__ float sc[2][64]; __shared__ float p[2][64];
  int g = blockIdx.x & 7, s = blockIdx.x >> 3, t = threadIdx.x;
  for (int i = t; i < 512; i += 256) qs[i >> 8][i & 255] = q[g * 512 + i];
  __syncthreads();
  const u16* K = kc + ((size_t)g * SK + (SK - W) + s * 64) * DH;
  const u16* V = vc + ((size_t)g * SK + (SK - W) + s * 64) * DH;
  if (t < 128) { int h = t >> 6, key = t & 63; float a = 0; for (int d = 0; d < DH; ++d) a += qs[h][d] * bf2f(K[key * DH + d]); sc[h][key] = mr(a) * 0.0625f; }
  __syncthreads();
  float m[2], l[2];
  for (int h = 0; h < 2; ++h) { float mm = -1e30f; for (int i = 0; i < 64; ++i) mm = fmaxf(mm, sc[h][i]); m[h] = mm; }
  if (t < 128) { int h = t >> 6, key = t & 63; p[h][key] = __expf(sc[h][key] - m[h]); }
  __syncthreads();
  for (int h = 0; h < 2; ++h) { float ll = 0; for (int i = 0; i < 64; ++i) ll += p[h][i]; l[h] = ll; }
  for (int h = 0; h < 2; ++h) { float a = 0; for (int i = 0; i < 64; ++i) a += p[h][i] * bf2f(V[i * DH + t]); po[((size_t)(g * 2 + h) * 32 + s) * DH + t] = a; }
  if (t < 2) { pml[((g * 2 + t) * 32 + s) * 2] = m[t]; pml[((g * 2 + t) * 32 + s) * 2 + 1] = l[t]; }
}
__global__ void k_comb(const float* po, const float* pml, float* o) {
  int h = blockIdx.x, t = threadIdx.x;
  float M = -1e30f; for (int s = 0; s < 32; ++s) M = fmaxf(M, pml[(h * 32 + s) * 2]);
  float L = 0, a = 0;
  for (int s = 0; s < 32; ++s) { float w = __expf(pml[(h * 32 + s) * 2] - M); L += w * pml[(h * 32 + s) * 2 + 1]; a += w * po[((size_t)h * 32 + s) * DH + t]; }
  o[h * DH + t] = rbf(a / L);
}
__global__ void k_gemv_res(const float* v, const u16* w, int K, float* x, u16* out) {
  __shared__ float red[16][64];
  int lane = threadIdx.x & 63, wv = threadIdx.x >> 6, col = blockIdx.x * 64 + lane;
  int kc = K / 16; float acc = 0;
  for (int k = wv * kc; k < wv * kc + kc; ++k) acc += v[k] * bf2f(w[(size_t)k * D + col]);
  red[wv][lane] = acc; __syncthreads();
  if (wv == 0) { float s = 0; for (int i = 0; i < 16; ++i) s += red[i][lane]; float r = mr(x[col] + mr(s)); x[col] = r; if (out) out[col] = (u16)(__float_as_uint(rbf(r)) >> 16); }
}
__global__ void k_swiglu(const float* gt, const float* up, float* h) { int i = threadIdx.x + blockIdx.x * blockDim.x; if (i < F) { float g = gt[i]; float si = mr(g / (1.f + __expf(-g))); h[i] = rbf(si * up[i]); } }

extern "C" void kernel_launch(void* const* d_in, const int* in_sizes, int n_in, void* d_out, int out_size, void* d_ws, size_t ws_size, hipStream_t stream) {
  const u16* ax = (const u16*)d_in[0]; const u16* kc = (const u16*)d_in[1]; const u16* vc = (const u16*)d_in[2];
  const u16* wq = (const u16*)d_in[3]; const u16* wo = (const u16*)d_in[4];
  const u16* w1 = (const u16*)d_in[6]; const u16* w3 = (const u16*)d_in[7]; const u16* w2 = (const u16*)d_in[8];
  float* ws = (float*)d_ws;
  float* x = ws; float* q = ws + 1024; float* o = q + 4096; float* gt = o + 4096; float* up = gt + 2048; float* h = up + 2048;
  float* pml = h + 2048; float* po = pml + 2048;
  k_init<<<2, 256, 0, stream>>>(ax, x);
  for (int l = 0; l < 2; ++l) {
    k_ln_gemv<<<64, 256, 0, stream>>>(x, wq, 4096, q);
    k_attn<<<256, 256, 0, stream>>>(q, kc, vc, po, pml);
    k_comb<<<16, 256, 0, stream>>>(po, pml, o);
    k_gemv_res<<<7, 1024, 0, stream>>>(o, wo, 4096, x, nullptr);
    k_ln_gemv<<<28, 256, 0, stream>>>(x, w1, F, gt);
    k_ln_gemv<<<28, 256, 0, stream>>>(x, w3, F, up);
    k_swiglu<<<7, 256, 0, stream>>>(gt, up, h);
    k_gemv_res<<<7, 1024, 0, stream>>>(h, w2, F, x, l == 1 ? (u16*)d_out : nullptr);
  }
}
```

```cpp
#include <hip/hip_runtime.h>
#include <stdint.h>
#include <type_traits>

namespace mk {
typedef unsigned u32; typedef unsigned short u16; typedef unsigned long long u64;
typedef u32 v4u __attribute__((ext_vector_type(4)));
typedef u32 v2u __attribute__((ext_vector_type(2)));
typedef float f2 __attribute__((ext_vector_type(2)));
typedef float f4 __attribute__((ext_vector_type(4)));
typedef short s4v __attribute__((ext_vector_type(4)));
typedef __bf16 bf2 __attribute__((ext_vector_type(2)));
typedef __bf16 bf8 __attribute__((ext_vector_type(8)));
#define LAS __attribute__((address_space(3)))
#define DEV __device__ __forceinline__

constexpr int DM = 448, SK = 8192, DH = 256, WIN = 2048, FF = 1792, NQ = 4096;
constexpr int NT = 512;
constexpr int WS_EP = 0;
constexpr int WS_RDY = 4096;
constexpr int W_Q = 8192, WQ_LAYER = 32768;
constexpr int W_C = W_Q + 2 * WQ_LAYER, WC_LAYER = 262144;
constexpr int W_ML = W_C + 2 * WC_LAYER, WML_LAYER = 4096;
#define DREP 8
constexpr int W_D = W_ML + 2 * WML_LAYER, WD_REP = 16384, WD_LAYER = 8 * WD_REP;
constexpr int W_H = W_D + 2 * WD_LAYER, WH_REP = 4096, WH_LAYER = 8 * WH_REP;
constexpr int W_A = W_H + 2 * WH_LAYER, WA_REP = 1024;
constexpr int WS_BYTES = W_A + 8 * WA_REP;
constexpr int WS_STAMP = 4 << 20;
constexpr u32 SENT = 0xFFFFFFFFu;
constexpr u32 OOB = 0x80000000u;

constexpr int S_WQ = 0;
constexpr int S_V = 14336;
constexpr int S_WO = S_V + 32768;
constexpr int S_W13 = S_WO + 32768;
constexpr int S_Q = S_W13 + 14336;
constexpr int S_SC = S_Q + 1280;
constexpr int S_P = S_SC + 640;
constexpr int S_E = S_P + 8 * 320;
constexpr int S_PART = S_E + 8 * 128;
constexpr int S_XB = S_PART + 2048;
constexpr int S_XR = S_XB + 1024;
constexpr int S_X1 = S_XR + 1024;
constexpr int S_HB = S_X1 + 1792;
constexpr int S_END = S_HB + 64;
constexpr int LDS_BYTES = (S_END + 512 + 255) / 256 * 256;
static_assert(LDS_BYTES > 81920 && LDS_BYTES <= 163840, "LDS must pin one workgroup per CU");

struct Params {
  const u16 *x, *k, *v, *wq, *wo, *w1, *w3, *w2;
  u16* out;
  unsigned char* ws;
};

#define SPIN_CAP (1u << 18)
#define BACKOFF 1
#define NTW 2
#define DL_B0 8
#define DL_B1 8
#define DL_C0 8
#define DL_C1 8
#define DL_D0 8
#define DL_D1 8
#define DL_DS 20
#define DL_H 20
#define DL_A 40
#define DL_AS 8

DEV float bflo(u32 v) { return __uint_as_float(v << 16); }
DEV float bfhi(u32 v) { return __uint_as_float(v & 0xFFFF0000u); }
DEV u32 cvtpk(float a, float b) { f2 v = {a, b}; bf2 r = __builtin_convertvector(v, bf2); return __builtin_bit_cast(u32, r); }
DEV float dot2(u32 a, u32 b, float c) { return __builtin_amdgcn_fdot2_f32_bf16(__builtin_bit_cast(bf2, a), __builtin_bit_cast(bf2, b), c, false); }
DEV f4 mfma(v4u a, v4u b, f4 c) { return __builtin_amdgcn_mfma_f32_16x16x32_bf16(__builtin_bit_cast(bf8, a), __builtin_bit_cast(bf8, b), c, 0, 0, 0); }
template <int C> DEV float dpp(float x) { return __builtin_bit_cast(float, __builtin_amdgcn_mov_dpp(__builtin_bit_cast(int, x), C, 0xf, 0xf, true)); }
constexpr int ROR8 = 0x128, ROR4 = 0x124, ROR2 = 0x122, ROR1 = 0x121, QX1 = 0xB1, QX2 = 0x4E, HMIR = 0x141;
DEV float sw32_add(float a, float b) { auto s = __builtin_amdgcn_permlane32_swap(__float_as_uint(a), __float_as_uint(b), false, false); return __uint_as_float(s[0]) + __uint_as_float(s[1]); }
DEV float sw16_add(float a, float b) { auto s = __builtin_amdgcn_permlane16_swap(__float_as_uint(a), __float_as_uint(b), false, false); return __uint_as_float(s[0]) + __uint_as_float(s[1]); }
DEV float sw32_max(float a, float b) { auto s = __builtin_amdgcn_permlane32_swap(__float_as_uint(a), __float_as_uint(b), false, false); return fmaxf(__uint_as_float(s[0]), __uint_as_float(s[1])); }
DEV float sw16_max(float a, float b) { auto s = __builtin_amdgcn_permlane16_swap(__float_as_uint(a), __float_as_uint(b), false, false); return fmaxf(__uint_as_float(s[0]), __uint_as_float(s[1])); }
DEV float row_sum(float z) { z += dpp<ROR8>(z); z += dpp<ROR4>(z); z += dpp<ROR2>(z); z += dpp<ROR1>(z); return z; }
DEV float row_max(float z) { z = fmaxf(z, dpp<ROR8>(z)); z = fmaxf(z, dpp<ROR4>(z)); z = fmaxf(z, dpp<ROR2>(z)); z = fmaxf(z, dpp<ROR1>(z)); return z; }
DEV float sum4(float a, float b, float c, float d) { return row_sum(sw16_add(sw32_add(a, c), sw32_add(b, d))); }
DEV float rdl(float v, int l) { return __uint_as_float(__builtin_amdgcn_readlane(__float_as_uint(v), l)); }
DEV void pin(u32& v) { asm volatile("" : "+v"(v)); }
DEV void pin(float& v) { asm volatile("" : "+v"(v)); }
#define SB() __builtin_amdgcn_sched_barrier(0)
#define BAR() do { asm volatile("s_waitcnt lgkmcnt(0)" ::: "memory"); __builtin_amdgcn_s_barrier(); asm volatile("" ::: "memory"); } while (0)
template <int N> DEV void slp() { asm volatile("" ::: "memory"); if (N > 0) __builtin_amdgcn_s_sleep(N); asm volatile("" ::: "memory"); SB(); }
#define CNT(i)
#define REPOLL(id, bad, LOADS, BAD) do { if (__builtin_expect(__any(bad), 0)) { for (u32 sp_ = 0; sp_ < SPIN_CAP; ++sp_) { asm volatile("" ::: "memory"); CNT(id); \
    if (BACKOFF) __builtin_amdgcn_s_sleep(BACKOFF); LOADS; if (!__any(BAD)) break; } } asm volatile("" ::: "memory"); } while (0)

typedef __amdgpu_buffer_rsrc_t rsrc_t;
DEV rsrc_t mkrs(const void* p, u32 bytes) { return __builtin_amdgcn_make_buffer_rsrc((void*)p, 0, bytes, 0x00020000); }
DEV v4u ldw(rsrc_t r, u32 off) { return __builtin_amdgcn_raw_buffer_load_b128(r, off, 0, NTW); }
DEV v4u ldx4(rsrc_t r, u32 off, u32 so) { return __builtin_amdgcn_raw_buffer_load_b128(r, off, so, 16); }
DEV v2u ldx2(rsrc_t r, u32 off, u32 so) { return __builtin_amdgcn_raw_buffer_load_b64(r, off, so, 16); }
DEV void stx4(v4u v, rsrc_t r, u32 off, u32 so) { __builtin_amdgcn_raw_buffer_store_b128(v, r, off, so, 16); }
DEV void stx2(v2u v, rsrc_t r, u32 off, u32 so) { __builtin_amdgcn_raw_buffer_store_b64(v, r, off, so, 16); }
DEV void stx1(u32 v, rsrc_t r, u32 off, u32 so) { __builtin_amdgcn_raw_buffer_store_b32(v, r, off, so, 16); }
DEV u32 vmax(v4u v) { return max(max(max(v.x, v.y), v.z), v.w); }

#define STAMP(i)
#define STAMP_FLUSH()

DEV float ln_proj(const v4u xp, const u32 (&wa)[4], const u32 (&wb)[4], float S) {
  const u32 ONE = 0x3F803F80u;
  float s1 = 0.f, s2 = 0.f, da = 0.f, db = 0.f;
#pragma unroll
  for (int i = 0; i < 4; ++i) { da = dot2(xp[i], wa[i], da); db = dot2(xp[i], wb[i], db); s1 = dot2(xp[i], ONE, s1); s2 = dot2(xp[i], xp[i], s2); }
  const float z = sum4(s1, s2, da, db);
  const float mu = rdl(z, 0) * (1.f / DM);
  const float var = fmaf(rdl(z, 16), 1.f / DM, -mu * mu);
  const float r = __builtin_amdgcn_rsqf(fmaxf(var, 0.f) + 1e-5f);
  return (z - mu * S) * r;
}

__global__ void __launch_bounds__(NT, 2) mega(Params p) {
  extern __shared__ __attribute__((aligned(16))) unsigned char lds_[];
  LAS unsigned char* L = (LAS unsigned char*)lds_;
  const int c = blockIdx.x, g = c & 7, j = c >> 3;
  const int t = threadIdx.x, l = t & 63, w = __builtin_amdgcn_readfirstlane(t >> 6);
  const int u = l >> 4, n = l & 15;
  const bool special = j >= 28, has_wo = j < 14;
  STAMP(0);
  if (j == 31 && w == 7) { asm volatile("buffer_wbl2 sc1" ::: "memory"); }
  const rsrc_t rws = mkrs(p.ws, WS_BYTES);

  const v4u ep = ldx4(rws, WS_EP, 0);
  v4u xp = __builtin_amdgcn_raw_buffer_load_b128(mkrs(p.x, DM * 2), l * 16, 0, 0);
  v4u wq_raw[2];
  { const rsrc_t rwq = mkrs(p.wq, DM * NQ * 2);
    const u32 o = (t >> 1) * (NQ * 2) + (g * 512 + j * 16) * 2 + (t & 1) * 16;
    wq_raw[0] = ldw(rwq, o); wq_raw[1] = ldw(rwq, o + 256 * (NQ * 2)); }
  SB(); __builtin_amdgcn_s_barrier(); SB();
  STAMP(12);
  const int hh = j >= 7, h = 2 * g + hh, cb = j - 7 * hh;
  const int ub = 28 * g + j;
  const int sp = 4 * g + (j - 28);
  const bool has_w2 = special && sp < 28;
  {
    const v4u S4 = {SENT, SENT, SENT, SENT};
    u32 o = OOB;
    if (w == 4 || w == 5) o = W_C + (w - 4) * WC_LAYER + ((2 * g + (l >> 5)) * 32 + j) * 512 + (l & 31) * 16;
    if (w >= 6 && has_wo && (l >> 3) < DREP) o = W_D + (w - 6) * WD_LAYER + (l >> 3) * WD_REP + h * 896 + cb * 128 + (l & 7) * 16;
    stx4(S4, rws, o, 0);
    STAMP(26);
    o = OOB;
    if (w == 5 && l < 16 && !special) o = W_H + (l >> 3) * WH_LAYER + (l & 7) * WH_REP + ub * 16;
    if (w == 6 && l < 16 && has_w2) o = W_A + (l >> 1) * WA_REP + sp * 32 + (l & 1) * 16;
    stx4(S4, rws, o, 0);
    STAMP(27);
    stx2(v2u{SENT, SENT}, rws, (w == 4 && l < 4) ? W_ML + (l >> 1) * WML_LAYER + ((2 * g + (l & 1)) * 32 + j) * 8 : OOB, 0);
  }
  SB();
  STAMP(28);
  v4u kf[8];
  const u32 kvbase = ((u32)g * SK + (SK - WIN) + j * 64) * (DH * 2);
  { const rsrc_t rk = mkrs(p.k, 8u * SK * DH * 2);
    const u32 ko = w < 4 ? kvbase + (16 * w + n) * (DH * 2) + u * 16 : OOB;
#pragma unroll
    for (int s = 0; s < 8; ++s) kf[s] = ldw(rk, ko + 64 * s); }
  SB();
  STAMP(13);
  u32 o_qs = l == 32 ? W_Q + (g * 32 + j) * 128 + w * 16 : OOB;
  u32 o_qg = t < 256 ? W_Q + g * 4096 + t * 16 : OOB;
  u32 o_cs = n < 2 ? W_C + ((2 * g + n) * 32 + j) * 512 + (32 * w + 8 * u) * 2 : OOB;
  u32 o_ms = (l & 31) == 0 ? W_ML + ((2 * g + (l >> 5)) * 32 + j) * 8 : OOB;
  const int hx = has_wo ? h : 2 * g;
  u32 o_cg = W_C + (hx * 32 + 16 * (w & 1) + n) * 512 + (64 * (w >> 1) + 8 * u) * 2;
  u32 o_mg = W_ML + (hx * 32 + (l & 31)) * 8;
  u32 o_ds = (l & 1) == 0 ? W_D + h * 896 + cb * 128 + (l >> 1) * 4 : OOB;
  u32 o_dg = w < 7 ? W_D + (DREP == 8 ? g * WD_REP : 0) + (l >> 3) * 896 + w * 128 + (l & 7) * 16 : OOB;
  u32 o_hs = l < 8 ? W_H + l * WH_REP + ub * 16 : OOB;
  u32 o_hg = w < 7 ? W_H + g * WH_REP + (32 * w + (l & 31)) * 16 : OOB;
  u32 o_as = W_A + (l >> 3) * WA_REP + sp * 32 + (l & 7) * 4;
  u32 o_ag = l < 56 ? W_A + g * WA_REP + l * 16 : OOB;
  pin(o_qs); pin(o_qg); pin(o_cs); pin(o_ms); pin(o_cg); pin(o_mg); pin(o_ds); pin(o_dg); pin(o_hs); pin(o_hg); pin(o_as); pin(o_ag);
  u32 dcol = 64 * w + 8 * (l & 7) + 4 * (l >> 5) + 2 * ((l >> 4) & 1) + ((l >> 3) & 1);
  pin(dcol);
  u32 a_wq[2];
  { const int i0 = t, i1 = t + 512; a_wq[0] = S_WQ + ((i0 ^ ((i0 >> 4) & 7)) * 16); a_wq[1] = S_WQ + ((i1 ^ ((i1 >> 4) & 7)) * 16); }
  const int lc = min(l, 55);
  u32 a_rd = S_WQ + (w & 3) * 4;
  SB();
  STAMP(14);
  *(LAS v4u*)(L + a_wq[0]) = wq_raw[0];
  if (t < 384) *(LAS v4u*)(L + a_wq[1]) = wq_raw[1];
  if (w == 0 && l < 56) *(LAS v4u*)(L + S_XR + l * 16) = xp;
  STAMP(15);
  asm volatile("s_waitcnt vmcnt(8)" ::: "memory");
  BAR();
  STAMP(11);
  const u32 T0 = ep.x + 1u, T1 = ep.y, T2 = ep.z;
  stx4(v4u{T0, T1, T2, 0u}, rws, t == 0 ? WS_RDY + c * 16 : OOB, 0);
  const u32 ONEM = l < 56 ? 0x3F803F80u : 0u;
  u32 wqa[4], wqb[4];
  float Sq;
  {
    u32 d[8];
#pragma unroll
    for (int jj = 0; jj < 8; ++jj) { const int slot = (16 * lc + 2 * jj + (w >> 2)) ^ (lc & 7); d[jj] = *(LAS u32*)(L + a_rd + slot * 16); }
    float a = 0.f, b = 0.f;
#pragma unroll
    for (int i = 0; i < 4; ++i) { wqa[i] = __builtin_amdgcn_perm(d[2 * i + 1], d[2 * i], 0x05040100u); wqb[i] = __builtin_amdgcn_perm(d[2 * i + 1], d[2 * i], 0x07060302u);
      a = dot2(wqa[i], ONEM, a); b = dot2(wqb[i], ONEM, b); }
    Sq = sum4(0.f, 0.f, a, b);
  }

  v4u vf[2][2];
  v4u wof[2][4];
  u32 w1a[4], w3a[4]; float S13 = 0.f;
  u32 w2r[8][4];
  v4u v_raw[4], wo_raw[4], w13_raw[2], w2_raw[8];

  auto layer = [&](auto LC) {
    constexpr int LY = decltype(LC)::value;
    constexpr u32 SQ = LY * WQ_LAYER, SC = LY * WC_LAYER, SM = LY * WML_LAYER, SD = LY * WD_LAYER, SH = LY * WH_LAYER;
    constexpr int SO = 16 * LY;
    {
      const float y = ln_proj(xp, wqa, wqb, Sq);
      auto s = __builtin_amdgcn_permlane16_swap(__float_as_uint(y), __float_as_uint(y), false, false);
      stx4(v4u{cvtpk(__uint_as_float(s[0]), __uint_as_float(s[1])), T0, T1, T2}, rws, o_qs, SQ);
    }
    STAMP(1 + SO);
    v4u rdy;
    slp<LY == 0 ? DL_B0 : DL_B1>();
    {
      v4u q = ldx4(rws, o_qg, SQ);
      if (LY == 0) {
        SB();
        rdy = ldx4(rws, t < 256 ? WS_RDY + t * 16 : OOB, 0);
        const rsrc_t rv = mkrs(p.v, 8u * SK * DH * 2);
#pragma unroll
        for (int i = 0; i < 4; ++i) v_raw[i] = ldw(rv, kvbase + (t + NT * i) * 16);
        SB();
      }
      const bool act = w < 4;
      bool bad = act & ((q.y != T0) | (q.z != T1) | (q.w != T2));
      REPOLL(32 + SO / 2, bad, q = ldx4(rws, o_qg, SQ), act & ((q.y != T0) | (q.z != T1) | (q.w != T2)));
      if (w < 4) *(LAS u32*)(L + S_Q + (t >> 7) * 576 + (t & 127) * 4) = q.x;
    }
    BAR();
    STAMP(2 + SO);
    if (LY == 0) {
      const rsrc_t rwo = mkrs(p.wo, DM * NQ * 2);
#pragma unroll
      for (int i = 0; i < 4; ++i) { const int idx = t + NT * i; wo_raw[i] = ldw(rwo, has_wo ? (h * 256 + (idx >> 3)) * (DM * 2) + cb * 128 + (idx & 7) * 16 : OOB); }
      SB();
    }
    if (w < 4) {
      f4 acc = {0.f, 0.f, 0.f, 0.f};
#pragma unroll
      for (int s = 0; s < 8; ++s) { const v4u qf = *(LAS v4u*)(L + S_Q + (n & 1) * 576 + s * 64 + u * 16); acc = mfma(kf[s], qf, acc); }
      if (n < 2) *(LAS f4*)(L + S_SC + n * 272 + (16 * w + 4 * u) * 4) = acc;
    }
    if (LY == 0) {
#pragma unroll
      for (int i = 0; i < 4; ++i) { const int idx = t + NT * i, row = idx >> 5, gr = idx & 31; *(LAS v4u*)(L + S_V + row * 512 + ((gr ^ (4 * (row & 3))) * 16)) = v_raw[i]; }
      const u32 ro = t < 256 ? WS_RDY + t * 16 : OOB; const bool act = w < 4;
      bool bad = act & ((rdy.x != T0) | (rdy.y != T1) | (rdy.z != T2));
      REPOLL(33, bad, rdy = ldx4(rws, ro, 0), act & ((rdy.x != T0) | (rdy.y != T1) | (rdy.z != T2)));
    }
    BAR();
    STAMP(3 + SO);
    if (LY == 0) {
#pragma unroll
      for (int tl = 0; tl < 2; ++tl)
#pragma unroll
        for (int st = 0; st < 2; ++st) {
          const int key = 32 * st + 8 * u + (n >> 2), gr = 4 * w + (n & 3);
          LAS unsigned char* a = L + S_V + key * 512 + ((gr ^ (4 * (key & 3))) * 16) + tl * 8;
          const v2u h0 = __builtin_bit_cast(v2u, __builtin_amdgcn_ds_read_tr16_b64_v4i16((LAS s4v*)a));
          const v2u h1 = __builtin_bit_cast(v2u, __builtin_amdgcn_ds_read_tr16_b64_v4i16((LAS s4v*)(a + 2048)));
          vf[tl][st] = v4u{h0.x, h0.y, h1.x, h1.y};
        }
    }
    {
      const float s0 = *(LAS float*)(L + S_SC + l * 4), s1 = *(LAS float*)(L + S_SC + 272 + l * 4);
      constexpr float CE = 0.0625f * 1.4426950408889634f;
      float m = row_max(sw32_max(s0, s1));
      m = sw16_max(m, m) * CE;
      auto ms = __builtin_amdgcn_permlane32_swap(__float_as_uint(m), __float_as_uint(m), false, false);
      const float p0 = __builtin_amdgcn_exp2f(fmaf(s0, CE, -__uint_as_float(ms[0]))), p1 = __builtin_amdgcn_exp2f(fmaf(s1, CE, -__uint_as_float(ms[1])));
      const u32 pk = cvtpk(p0, p1);
      *(LAS u16*)(L + S_P + w * 320 + l * 2) = (u16)pk;
      *(LAS u16*)(L + S_P + w * 320 + 160 + l * 2) = (u16)(pk >> 16);
      const v4u pf0 = *(LAS v4u*)(L + S_P + w * 320 + (n & 1) * 160 + u * 16), pf1 = *(LAS v4u*)(L + S_P + w * 320 + (n & 1) * 160 + 64 + u * 16);
      f4 o0 = {0.f, 0.f, 0.f, 0.f}, o1 = o0;
      o0 = mfma(vf[0][0], pf0, o0); o1 = mfma(vf[1][0], pf0, o1);
      o0 = mfma(vf[0][1], pf1, o0); o1 = mfma(vf[1][1], pf1, o1);
      stx4(v4u{cvtpk(o0[0], o0[1]), cvtpk(o0[2], o0[3]), cvtpk(o1[0], o1[1]), cvtpk(o1[2], o1[3])}, rws, o_cs, SC);
      if (w == 0) {
        float ls = row_sum(sw32_add(p0, p1)); ls = sw16_add(ls, ls);
        stx2(v2u{__float_as_uint(m), __float_as_uint(ls)}, rws, o_ms, SM);
      }
    }
    STAMP(4 + SO);
    slp<LY == 0 ? DL_C0 : DL_C1>();
    if (has_wo) {
      v2u ml = ldx2(rws, o_mg, SM);
      v4u a0 = ldx4(rws, o_cg, SC), a1 = ldx4(rws, o_cg + 64, SC);
      if (LY == 0) {
        SB();
        w13_raw[0] = ldw(mkrs(p.w1, DM * FF * 2), t * (FF * 2) + ub * 16);
        w13_raw[1] = ldw(mkrs(p.w3, DM * FF * 2), t * (FF * 2) + ub * 16);
        SB();
#pragma unroll
        for (int i = 0; i < 4; ++i) *(LAS v4u*)(L + S_WO + (t + NT * i) * 16) = wo_raw[i];
        BAR();
#pragma unroll
        for (int ks = 0; ks < 2; ++ks)
#pragma unroll
          for (int ct = 0; ct < 4; ++ct) {
            LAS unsigned char* a = L + S_WO + (64 * (w >> 1) + 32 * ks + 8 * u + (n >> 2)) * 128 + ct * 32 + (n & 3) * 8;
            const v2u h0 = __builtin_bit_cast(v2u, __builtin_amdgcn_ds_read_tr16_b64_v4i16((LAS s4v*)a));
            const v2u h1 = __builtin_bit_cast(v2u, __builtin_amdgcn_ds_read_tr16_b64_v4i16((LAS s4v*)(a + 512)));
            wof[ks][ct] = v4u{h0.x, h0.y, h1.x, h1.y};
          }
      }
      bool bad = max(max(vmax(a0), vmax(a1)), max(ml.x, ml.y)) == SENT;
      REPOLL(34 + SO / 2, bad, (ml = ldx2(rws, o_mg, SM), a0 = ldx4(rws, o_cg, SC), a1 = ldx4(rws, o_cg + 64, SC)), max(max(vmax(a0), vmax(a1)), max(ml.x, ml.y)) == SENT);
      STAMP(5 + SO);
      f4 d[4];
#pragma unroll
      for (int ct = 0; ct < 4; ++ct) d[ct] = mfma(a0, wof[0][ct], f4{0.f, 0.f, 0.f, 0.f});
#pragma unroll
      for (int ct = 0; ct < 4; ++ct) d[ct] = mfma(a1, wof[1][ct], d[ct]);
      const float mv = __uint_as_float(ml.x);
      float M = row_max(mv); M = sw16_max(M, M);
      const float e = __builtin_amdgcn_exp2f(mv - M);
      *(LAS float*)(L + S_E + w * 128 + (l & 31) * 4) = e;
      const f4 ev = *(LAS f4*)(L + S_E + w * 128 + (16 * (w & 1) + 4 * u) * 4);
      float Ls = row_sum(e * __uint_as_float(ml.y)); Ls = sw16_add(Ls, Ls);
      float cs[4];
#pragma unroll
      for (int ct = 0; ct < 4; ++ct) cs[ct] = ev[0] * d[ct][0] + ev[1] * d[ct][1] + ev[2] * d[ct][2] + ev[3] * d[ct][3];
      const float z = sw16_add(sw32_add(cs[0], cs[2]), sw32_add(cs[1], cs[3]));
      *(LAS float*)(L + S_PART + w * 256 + l * 4) = z;
      BAR();
      if (w == 0) {
        float s = 0.f;
#pragma unroll
        for (int i = 0; i < 8; ++i) s += *(LAS float*)(L + S_PART + i * 256 + l * 4);
        s *= __builtin_amdgcn_rcpf(Ls);
        const u32 pk = cvtpk(s, dpp<QX1>(s));
#pragma unroll
        for (int r = 0; r < DREP; ++r) stx1(pk, rws, o_ds, SD + r * WD_REP);
      }
      STAMP(6 + SO);
      slp<LY == 0 ? DL_D0 : DL_D1>();
    } else {
      v2u ml = ldx2(rws, o_mg, SM);
      if (LY == 0) {
        SB();
        if (special) {
          const rsrc_t rw2 = mkrs(p.w2, DM * FF * 2);
#pragma unroll
          for (int jj = 0; jj < 8; ++jj) w2_raw[jj] = ldw(rw2, (w < 7 && has_w2) ? (8 * (32 * w + (l & 31)) + jj) * (DM * 2) + sp * 32 + (l >> 5) * 16 : OOB);
        } else {
          w13_raw[0] = ldw(mkrs(p.w1, DM * FF * 2), t * (FF * 2) + ub * 16);
          w13_raw[1] = ldw(mkrs(p.w3, DM * FF * 2), t * (FF * 2) + ub * 16);
        }
        SB();
      }
      bool bad = max(ml.x, ml.y) == SENT;
      REPOLL(34 + SO / 2, bad, ml = ldx2(rws, o_mg, SM), max(ml.x, ml.y) == SENT);
      STAMP(5 + SO);
      slp<DL_DS>();
    }
    {
      v4u a = ldx4(rws, o_dg, SD), b = ldx4(rws, o_dg + 8 * 896, SD);
      const float xres = bflo(*(LAS u16*)(L + S_XR + (w < 7 ? dcol : 0) * 2));
      if (LY == 0) {
        if (!special) {
          if (t < 448) {
#pragma unroll
            for (int i = 0; i < 2; ++i) *(LAS v4u*)(L + S_W13 + i * 7168 + ((t ^ ((t >> 3) & 7)) * 16)) = w13_raw[i];
          }
          BAR();
          u32 d1[8], d3[8];
#pragma unroll
          for (int jj = 0; jj < 8; ++jj) { const int slot = (8 * lc + jj) ^ (lc & 7);
            d1[jj] = *(LAS u32*)(L + S_W13 + slot * 16 + (w >> 1) * 4); d3[jj] = *(LAS u32*)(L + S_W13 + 7168 + slot * 16 + (w >> 1) * 4); }
          const u32 sel = (w & 1) ? 0x07060302u : 0x05040100u;
          float a_ = 0.f, b_ = 0.f;
#pragma unroll
          for (int i = 0; i < 4; ++i) { w1a[i] = __builtin_amdgcn_perm(d1[2 * i + 1], d1[2 * i], sel); w3a[i] = __builtin_amdgcn_perm(d3[2 * i + 1], d3[2 * i], sel);
            a_ = dot2(w1a[i], ONEM, a_); b_ = dot2(w3a[i], ONEM, b_); }
          S13 = sum4(0.f, 0.f, a_, b_);
        } else {
#pragma unroll
          for (int q4 = 0; q4 < 4; ++q4)
#pragma unroll
            for (int i = 0; i < 4; ++i) {
              w2r[2 * q4][i] = __builtin_amdgcn_perm(w2_raw[2 * i + 1][q4], w2_raw[2 * i][q4], 0x05040100u);
              w2r[2 * q4 + 1][i] = __builtin_amdgcn_perm(w2_raw[2 * i + 1][q4], w2_raw[2 * i][q4], 0x07060302u);
            }
        }
      }
      bool bad = max(vmax(a), vmax(b)) == SENT;
      REPOLL(35 + SO / 2, bad, (a = ldx4(rws, o_dg, SD), b = ldx4(rws, o_dg + 8 * 896, SD)), max(vmax(a), vmax(b)) == SENT);
      STAMP(7 + SO);
      u32 LO = 0x00003F80u, HI = 0x3F800000u;
      asm volatile("" : "+s"(LO), "+s"(HI));
      float v8[8];
#pragma unroll
      for (int i = 0; i < 4; ++i) { v8[2 * i] = dot2(b[i], LO, dot2(a[i], LO, 0.f)); v8[2 * i + 1] = dot2(b[i], HI, dot2(a[i], HI, 0.f)); }
      float v4_[4], v2_[2];
#pragma unroll
      for (int i = 0; i < 4; ++i) v4_[i] = sw32_add(v8[i], v8[i + 4]);
#pragma unroll
      for (int i = 0; i < 2; ++i) v2_[i] = sw16_add(v4_[i], v4_[i + 2]);
      const bool k8 = l & 8;
      const float x1 = xres + (k8 ? v2_[1] : v2_[0]) + dpp<ROR8>(k8 ? v2_[0] : v2_[1]);
      if (w < 7) {
        if (!special) *(LAS u16*)(L + S_XB + dcol * 2) = (u16)cvtpk(x1, x1);
        else *(LAS float*)(L + S_X1 + dcol * 4) = x1;
      }
      BAR();
    }
    if (!special) {
      v4u xb = *(LAS v4u*)(L + S_XB + lc * 16);
      if (l >= 56) xb = v4u{0, 0, 0, 0};
      const float y = ln_proj(xb, w1a, w3a, S13);
      auto s = __builtin_amdgcn_permlane16_swap(__float_as_uint(y), __float_as_uint(y), false, false);
      const float gt = __uint_as_float(s[0]), up = __uint_as_float(s[1]);
      const float hv = gt * __builtin_amdgcn_rcpf(1.f + __builtin_amdgcn_exp2f(-1.4426950408889634f * gt)) * up;
      if (l == 32) *(LAS u16*)(L + S_HB + w * 2) = (u16)cvtpk(hv, hv);
      BAR();
      if (w == 0) stx4(*(LAS v4u*)(L + S_HB), rws, o_hs, SH);
      STAMP(8 + SO);
      if (LY == 1) return;
      slp<DL_A>();
    } else {
      if (has_w2) {
        slp<DL_H>();
        v4u hv = ldx4(rws, o_hg, SH);
        bool bad = vmax(hv) == SENT;
        REPOLL(36 + SO / 2, bad, hv = ldx4(rws, o_hg, SH), vmax(hv) == SENT);
        STAMP(8 + SO);
        float a[8];
#pragma unroll
        for (int cc = 0; cc < 8; ++cc) { float s = 0.f;
#pragma unroll
          for (int i = 0; i < 4; ++i) s = dot2(hv[i], w2r[cc][i], s);
          a[cc] = s; }
        float b4[4], b2[2];
#pragma unroll
        for (int i = 0; i < 4; ++i) b4[i] = sw16_add(a[i], a[i + 4]);
        { const bool k8 = l & 8;
#pragma unroll
          for (int i = 0; i < 2; ++i) b2[i] = (k8 ? b4[i + 2] : b4[i]) + dpp<ROR8>(k8 ? b4[i] : b4[i + 2]); }
        const bool k4 = l & 4;
        float z = (k4 ? b2[1] : b2[0]) + dpp<HMIR>(k4 ? b2[0] : b2[1]);
        z += dpp<QX1>(z); z += dpp<QX2>(z);
        if ((l & 3) == 0) *(LAS float*)(L + S_PART + w * 64 + ((l >> 5) * 8 + ((l >> 4) & 1) * 4 + ((l >> 3) & 1) * 2 + ((l >> 2) & 1)) * 4) = z;
        BAR();
        if (w == 0) {
          f2 s = *(LAS f2*)(L + S_PART + (l >> 3) * 64 + (l & 7) * 8);
          const f2 r = *(LAS f2*)(L + S_X1 + (16 * sp + 2 * (l & 7)) * 4);
          s.x += dpp<ROR8>(s.x); s.y += dpp<ROR8>(s.y);
          s.x = sw16_add(s.x, s.x); s.y = sw16_add(s.y, s.y);
          s.x = sw32_add(s.x, s.x); s.y = sw32_add(s.y, s.y);
          const u32 pk = cvtpk(s.x + r.x, s.y + r.y);
          if (LY == 0) stx1(pk, rws, o_as, 0);
          else {
            if (l < 8) *(u32*)(p.out + 16 * sp + 2 * l) = pk;
            if (c == 8 * 28 && l == 0) stx1(T0, rws, WS_EP, 0);
          }
        }
        STAMP(9 + SO);
      }
      if (LY == 1) return;
      slp<DL_AS>();
    }
    {
      v4u a = ldx4(rws, o_ag, 0);
      bool bad = vmax(a) == SENT;
      REPOLL(37, bad, a = ldx4(rws, o_ag, 0), vmax(a) == SENT);
      STAMP(10 + SO);
      xp = a;
      if (w == 0 && l < 56) *(LAS v4u*)(L + S_XR + l * 16) = a;
    }
  };
  layer(std::integral_constant<int, 0>{});
  layer(std::integral_constant<int, 1>{});
  STAMP_FLUSH();
}
}

extern "C" void kernel_launch(void* const* d_in, const int* in_sizes, int n_in, void* d_out, int out_size, void* d_ws, size_t ws_size, hipStream_t stream) {
  mk::Params p;
  p.x = (const mk::u16*)d_in[0]; p.k = (const mk::u16*)d_in[1]; p.v = (const mk::u16*)d_in[2];
  p.wq = (const mk::u16*)d_in[3]; p.wo = (const mk::u16*)d_in[4];
  p.w1 = (const mk::u16*)d_in[6]; p.w3 = (const mk::u16*)d_in[7]; p.w2 = (const mk::u16*)d_in[8];
  p.out = (mk::u16*)d_out; p.ws = (unsigned char*)d_ws;
  (void)hipFuncSetAttribute((const void*)mk::mega, hipFuncAttributeMaxDynamicSharedMemorySize, mk::LDS_BYTES);
  hipLaunchKernelGGL(mk::mega, dim3(256), dim3(mk::NT), mk::LDS_BYTES, stream, p);
}
```

```cpp
#include <hip/hip_runtime.h>
#include <stdint.h>
#include <type_traits>

namespace mk {
typedef unsigned u32; typedef unsigned short u16; typedef unsigned long long u64;
typedef u32 v4u __attribute__((ext_vector_type(4)));
typedef u32 v2u __attribute__((ext_vector_type(2)));
typedef float f2 __attribute__((ext_vector_type(2)));
typedef float f4 __attribute__((ext_vector_type(4)));
typedef short s4v __attribute__((ext_vector_type(4)));
typedef __bf16 bf2 __attribute__((ext_vector_type(2)));
typedef __bf16 bf8 __attribute__((ext_vector_type(8)));
#define LAS __attribute__((address_space(3)))
#define DEV __device__ __forceinline__

constexpr int DM = 448, SK = 8192, DH = 256, WIN = 2048, FF = 1792, NQ = 4096;
constexpr int NT = 512;
constexpr int WS_EP = 0;
constexpr int WS_RDY = 4096;
constexpr int W_Q0 = 8192, WQ0_GROUP = 8192, W_Q1 = W_Q0 + 8 * WQ0_GROUP, WQ1_GROUP = 4096;
constexpr int W_C = W_Q1 + 8 * WQ1_GROUP, WC_LAYER = 262144;
constexpr int W_ML0 = W_C + 2 * WC_LAYER, W_ML1 = W_ML0 + 65536;
constexpr int W_D = W_ML1 + 4096, WD_REP = 16384, WD_LAYER = 8 * WD_REP;
constexpr int W_H = W_D + 2 * WD_LAYER, WH_LAYER = 8192, WH_UP = 4096;
constexpr int W_A = W_H + 2 * WH_LAYER, WA_REP = 8192, WA_LAYER = 8 * WA_REP;
constexpr int WS_BYTES = W_A + 2 * WA_LAYER;
constexpr int WS_STAMP = 4 << 20;
constexpr u32 SENT = 0xFFFFFFFFu;
constexpr u32 OOB = 0x80000000u;

constexpr int S_WQ = 0;
constexpr int S_K = S_WQ + 16384;
constexpr int S_V = S_K + 32768;
constexpr int S_WX = S_V + 32768;
constexpr int S_Q = S_WX + 32768;
constexpr int S_SC = S_Q + 1280;
constexpr int S_P = S_SC + 640;
constexpr int S_E = S_P + 8 * 320;
constexpr int S_PART = S_E + 8 * 128;
constexpr int S_XB = S_PART + 4096;
constexpr int S_X1 = S_XB + 1024;
constexpr int S_HB = S_X1 + 1792;
constexpr int S_CNT = S_HB + 32;
constexpr int S_EP = S_HB + 48;
constexpr int S_TML = S_HB + 64;
constexpr int S_END = S_HB + 80;
constexpr int LDS_BYTES = (S_END + 255) / 256 * 256;
static_assert(LDS_BYTES > 81920 && LDS_BYTES <= 163840, "LDS must pin one workgroup per CU");

struct Params {
  const u16 *x, *k, *v, *wq, *wo, *w1, *w3, *w2;
  u16* out;
  unsigned char* ws;
};

#define SPIN_CAP (1u << 18)
#define BACKOFF 1
#define NTW 2
#define WARM 1
#define WARM_AUX 2
enum { D_B0, D_TB0, D_B1, D_C0, D_C1, D_DS0, D_DS1, D_H0, D_H1, D_A1, D_A2, D_A3, D_F, D_C0B, D_N };
#define DL_DEFAULT {6, 340, 12, 6, 12, 60, 65, 30, 30, 70, 50, 8, 6, 1}
#define WOE 8
#define WFE 4
#define DLV(k) dl_default[k]
constexpr int dl_default[D_N] = DL_DEFAULT;
#define DLY(k) dly(dl_default[k])

DEV float bflo(u32 v) { return __uint_as_float(v << 16); }
DEV u32 cvtpk(float a, float b) { f2 v = {a, b}; bf2 r = __builtin_convertvector(v, bf2); return __builtin_bit_cast(u32, r); }
DEV float dot2(u32 a, u32 b, float c) { return __builtin_amdgcn_fdot2_f32_bf16(__builtin_bit_cast(bf2, a), __builtin_bit_cast(bf2, b), c, false); }
DEV f4 mfma(v4u a, v4u b, f4 c) { return __builtin_amdgcn_mfma_f32_16x16x32_bf16(__builtin_bit_cast(bf8, a), __builtin_bit_cast(bf8, b), c, 0, 0, 0); }
template <int C> DEV float dpp(float x) { return __builtin_bit_cast(float, __builtin_amdgcn_mov_dpp(__builtin_bit_cast(int, x), C, 0xf, 0xf, true)); }
constexpr int ROR8 = 0x128, ROR4 = 0x124, ROR2 = 0x122, ROR1 = 0x121, QX1 = 0xB1, QX2 = 0x4E, HMIR = 0x141;
DEV float sw32_add(float a, float b) { auto s = __builtin_amdgcn_permlane32_swap(__float_as_uint(a), __float_as_uint(b), false, false); return __uint_as_float(s[0]) + __uint_as_float(s[1]); }
DEV float sw16_add(float a, float b) { auto s = __builtin_amdgcn_permlane16_swap(__float_as_uint(a), __float_as_uint(b), false, false); return __uint_as_float(s[0]) + __uint_as_float(s[1]); }
DEV float vmaxf(float a, float b) { return __builtin_amdgcn_fmed3f(a, b, __builtin_inff()); }
DEV float sw32_max(float a, float b) { auto s = __builtin_amdgcn_permlane32_swap(__float_as_uint(a), __float_as_uint(b), false, false); return vmaxf(__uint_as_float(s[0]), __uint_as_float(s[1])); }
DEV float sw16_max(float a, float b) { auto s = __builtin_amdgcn_permlane16_swap(__float_as_uint(a), __float_as_uint(b), false, false); return vmaxf(__uint_as_float(s[0]), __uint_as_float(s[1])); }
DEV float row_sum(float z) { z += dpp<ROR8>(z); z += dpp<ROR4>(z); z += dpp<ROR2>(z); z += dpp<ROR1>(z); return z; }
DEV float row_max(float z) { z = vmaxf(z, dpp<ROR8>(z)); z = vmaxf(z, dpp<ROR4>(z)); z = vmaxf(z, dpp<ROR2>(z)); z = vmaxf(z, dpp<ROR1>(z)); return z; }
DEV float sum4(float a, float b, float c, float d) { return row_sum(sw16_add(sw32_add(a, c), sw32_add(b, d))); }
DEV float rdl(float v, int l) { return __uint_as_float(__builtin_amdgcn_readlane(__float_as_uint(v), l)); }
DEV void pin(u32& v) { asm volatile("" : "+v"(v)); }
#define SB() __builtin_amdgcn_sched_barrier(0)
#define BAR() do { asm volatile("s_waitcnt lgkmcnt(0)" ::: "memory"); __builtin_amdgcn_s_barrier(); asm volatile("" ::: "memory"); } while (0)
DEV void wait_until(u64 t) { asm volatile("" ::: "memory"); while (__builtin_amdgcn_s_memrealtime() < t) {} asm volatile("" ::: "memory"); SB(); }
DEV void dly(int n) {
  asm volatile("" ::: "memory");
  for (int i = 0; i < n; ++i) __builtin_amdgcn_s_sleep(1);
  asm volatile("" ::: "memory"); SB();
}
#define CNT(i)
#define STAMP(i)
#define STAMP_FLUSH()
#define RDY_CHECK() do { if (LO) { v4u rdy = ldx4(rws, WS_RDY + t * 16, 0); bool bad_ = (rdy.x != R0) | (rdy.y != R1) | (rdy.z != R2) | (rdy.w != R3); \
    REPOLL(33, bad_, rdy = ldx4(rws, WS_RDY + t * 16, 0), (rdy.x != R0) | (rdy.y != R1) | (rdy.z != R2) | (rdy.w != R3)); } } while (0)
#define REPOLL(id, bad, LOADS, BAD) do { if (__builtin_expect(__any(bad), 0)) { for (u32 sp_ = 0; sp_ < SPIN_CAP; ++sp_) { asm volatile("" ::: "memory"); CNT(id); \
    if (BACKOFF) __builtin_amdgcn_s_sleep(BACKOFF); LOADS; if (!__any(BAD)) break; } } asm volatile("" ::: "memory"); } while (0)

typedef __amdgpu_buffer_rsrc_t rsrc_t;
DEV rsrc_t mkrs(const void* p, u32 bytes) { return __builtin_amdgcn_make_buffer_rsrc((void*)p, 0, bytes, 0x00020000); }
DEV v4u ldw(rsrc_t r, u32 off) { return __builtin_amdgcn_raw_buffer_load_b128(r, off, 0, NTW); }
DEV v4u ldx4(rsrc_t r, u32 off, u32 so) { return __builtin_amdgcn_raw_buffer_load_b128(r, off, so, 16); }
DEV v2u ldx2(rsrc_t r, u32 off, u32 so) { return __builtin_amdgcn_raw_buffer_load_b64(r, off, so, 16); }
DEV u32 ldx1(rsrc_t r, u32 off, u32 so) { return __builtin_amdgcn_raw_buffer_load_b32(r, off, so, 16); }
DEV void stx4(v4u v, rsrc_t r, u32 off, u32 so) { __builtin_amdgcn_raw_buffer_store_b128(v, r, off, so, 16); }
DEV void stx2(v2u v, rsrc_t r, u32 off, u32 so) { __builtin_amdgcn_raw_buffer_store_b64(v, r, off, so, 16); }
DEV void stx1(u32 v, rsrc_t r, u32 off, u32 so) { __builtin_amdgcn_raw_buffer_store_b32(v, r, off, so, 16); }
DEV u32 vmax(v4u v) { return max(max(max(v.x, v.y), v.z), v.w); }
constexpr u32 ONE = 0x3F803F80u;

DEV void read_cols(LAS unsigned char* base, int lc, int w, v4u& wa, v4u& wb) {
  u32 d[8];
#pragma unroll
  for (int jj = 0; jj < 8; ++jj) d[jj] = *(LAS u32*)(base + (((16 * lc + 2 * jj + (w >> 2)) ^ (lc & 7)) * 16) + (w & 3) * 4);
#pragma unroll
  for (int i = 0; i < 4; ++i) { wa[i] = __builtin_amdgcn_perm(d[2 * i + 1], d[2 * i], 0x05040100u); wb[i] = __builtin_amdgcn_perm(d[2 * i + 1], d[2 * i], 0x07060302u); }
}
DEV float dot8(const v4u x, const v4u wv) { float a = 0.f;
#pragma unroll
  for (int i = 0; i < 4; ++i) a = dot2(x[i], wv[i], a);
  return a; }
DEV float rows4(float a, float b, float c, float d) { return row_sum(sw16_add(sw32_add(a, c), sw32_add(b, d))); }
DEV void ln_stats(const v4u x, float& mu, float& r) {
  float st = sw32_add(dot8(x, v4u{ONE, ONE, ONE, ONE}), dot8(x, x)); st = row_sum(sw16_add(st, st));
  mu = rdl(st, 0) * (1.f / DM);
  r = __builtin_amdgcn_rsqf(fmaxf(fmaf(rdl(st, 32), 1.f / DM, -mu * mu), 0.f) + 1e-5f);
}
DEV float reduce_rows(const float (&v8)[8], int l) {
  float v4_[4], v2_[2];
#pragma unroll
  for (int i = 0; i < 4; ++i) v4_[i] = sw32_add(v8[i], v8[i + 4]);
#pragma unroll
  for (int i = 0; i < 2; ++i) v2_[i] = sw16_add(v4_[i], v4_[i + 2]);
  const bool k8 = l & 8;
  return (k8 ? v2_[1] : v2_[0]) + dpp<ROR8>(k8 ? v2_[0] : v2_[1]);
}
template <int N, int I = 0, class F> DEV void static_for(F&& f) { if constexpr (I < N) { f(std::integral_constant<int, I>{}); static_for<N, I + 1>(f); } }
DEV float split_weights(const v2u ml, LAS unsigned char* E, int l) {
  const float mv = __uint_as_float(ml.x);
  float M = row_max(mv); M = sw16_max(M, M);
  const float e = __builtin_amdgcn_exp2f(mv - M);
  *(LAS float*)(E + (l & 31) * 4) = e;
  return e;
}
DEV float split_norm(float e, const v2u ml) { float Ls = row_sum(e * __uint_as_float(ml.y)); return sw16_add(Ls, Ls); }
DEV float bfhi(u32 v) { return __uint_as_float(v & 0xFFFF0000u); }

template <int ROLE, bool LO>
DEV void program(const Params& p, LAS unsigned char* L, const int c, const int t, const int w) {
  const int g = c & 7, j = c >> 3, l = t & 63, u = l >> 4, n = l & 15;
  asm volatile("; start of program %0 %1" :: "n"(ROLE), "n"(LO ? 1 : 0) : "memory");
  STAMP(0);
  const u64 t_start = LO ? __builtin_amdgcn_s_memrealtime() : 0;
  const rsrc_t rws = mkrs(p.ws, WS_BYTES);
  const int hh = j >= 7, h = 2 * g + hh, cb = j - 7 * hh;
  const bool isup = j >= 21;
  const int ub = 7 * g + (j - (isup ? 21 : 14));
  const int wa = j - 28;
  const int kq = j >> 3, cq = j & 7;
  const u32 kvbase = ((u32)g * SK + (SK - WIN) + j * 64) * (DH * 2);

  v4u xp = {0, 0, 0, 0}, ep = {0, 0, 0, 0}, wq_raw[4];
  if constexpr (LO) {
    const rsrc_t rwq = mkrs(p.wq, DM * NQ * 2);
    const u32 o = (112 * kq + (t >> 3)) * (NQ * 2) + (g * 512 + cq * 64) * 2 + (t & 7) * 16;
#pragma unroll
    for (int i = 0; i < 4; ++i) wq_raw[i] = ldw(rwq, (i < 3 || t < 128) ? o + i * 32 * (NQ * 2) : OOB);
    xp = __builtin_amdgcn_raw_buffer_load_b128(mkrs(p.x, DM * 2), w == 0 ? l * 16 : OOB, 0, 0);
    ep = ldx4(rws, (w == 0 && l == 0) ? WS_EP : OOB, 0);
    SB();
    const v4u S4 = {SENT, SENT, SENT, SENT};
    u32 oc = OOB;
    if (w < 2) oc = W_C + w * WC_LAYER + g * 32768 + j * 1024 + l * 16;
    else if (ROLE == 0) oc = W_D + (w - 2) * WD_LAYER + (l >> 3) * WD_REP + h * 896 + cb * 128 + (l & 7) * 16;
    else if (ROLE == 2) oc = ((l & 15) + 16 * wa < 56) ? W_A + (w - 2) * WA_LAYER + (l >> 4) * WA_REP + g * 896 + wa * 256 + (l & 15) * 16 : OOB;
    stx4(S4, rws, oc, 0);
    oc = OOB;
    if (w == 0 && l < 8) oc = W_Q1 + g * WQ1_GROUP + kq * 1024 + cq * 128 + l * 16;
    if (ROLE == 1 && w == 1 && l < 8) oc = W_H + (l >> 2) * WH_LAYER + (isup ? WH_UP : 0) + ub * 64 + (l & 3) * 16;
    if (ROLE == 2 && w >= 2 && (l & 15) + 16 * wa < 56) oc = W_A + (w - 2) * WA_LAYER + (4 + (l >> 4)) * WA_REP + g * 896 + wa * 256 + (l & 15) * 16;
    stx4(S4, rws, oc, 0);
    oc = OOB;
    if (w == 0 && l < 2) oc = W_ML0 + ((2 * g + l) * 32 + j) * 128;
    if (w == 0 && j == 0 && l >= 32) oc = W_ML1 + (2 * g + ((l >> 4) & 1)) * 256 + (l & 15) * 16;
    stx4(S4, rws, oc, 0);
    if (t == 0) *(LAS v2u*)(L + S_CNT) = v2u{0u, 0u};
    asm volatile("s_waitcnt lgkmcnt(0)" ::: "memory");
  }
  SB();
  __builtin_amdgcn_s_barrier();
  SB();
  STAMP(12);
  v4u k_raw[8], v_raw[8], wx_raw[8];
  if constexpr (LO) {
    const rsrc_t rk = mkrs(p.k, 8u * SK * DH * 2);
#pragma unroll
    for (int i = 0; i < 8; ++i) k_raw[i] = ldw(rk, kvbase + (t + 256 * i) * 16);
  } else {
    const int tt = t - 256;
    const rsrc_t rv = mkrs(p.v, 8u * SK * DH * 2);
#pragma unroll
    for (int i = 0; i < 8; ++i) v_raw[i] = ldw(rv, kvbase + (tt + 256 * i) * 16);
    STAMP(29);
  }
  SB();
  STAMP(13);
  u32 o_qs0 = n == 0 ? W_Q0 + g * WQ0_GROUP + kq * 2048 + (16 * cq + 4 * w + u) * 16 : OOB;
  u32 o_qs1 = n == 0 ? W_Q1 + g * WQ1_GROUP + kq * 1024 + (64 * cq + 16 * w + 4 * u) * 2 : OOB;
  u32 o_qg0 = W_Q0 + g * WQ0_GROUP + (l >> 5) * 4096 + (32 * w + (l & 31)) * 16;
  u32 o_qg1 = W_Q1 + g * WQ1_GROUP + (l >> 5) * 2048 + (32 * w + (l & 31)) * 8;
  u32 o_cs = n < 2 ? W_C + (2 * g + n) * 16384 + (j >> 4) * 8192 + w * 1024 + (u * 16 + (j & 15)) * 16 : OOB;
  u32 o_ms0 = (l & 31) == 0 ? W_ML0 + ((2 * g + (l >> 5)) * 32 + j) * 128 : OOB, o_ms1 = (l & 31) == 0 ? W_ML1 + ((2 * g + (l >> 5)) * 32 + j) * 8 : OOB;
  const int hx = ROLE == 0 ? h : 2 * g;
  u32 o_cg = W_C + hx * 16384 + (w & 1) * 8192 + (w >> 1) * 2048 + l * 16;
  u32 o_mg0 = W_ML0 + (hx * 32 + (l & 31)) * 128, o_mg1 = W_ML1 + (hx * 32 + (l & 31)) * 8;
  u32 o_pub, o_gat = OOB;
  if (ROLE == 0) o_pub = (l & 1) == 0 ? W_D + h * 896 + cb * 128 + (l >> 1) * 4 : OOB;
  else if (ROLE == 1) o_pub = l == 0 ? W_H + (isup ? WH_UP : 0) + ub * 64 + w * 8 : OOB;
  else { o_pub = 128 * wa + 2 * l < DM ? W_A + g * 896 + wa * 256 + l * 4 : OOB;
         o_gat = w < 7 ? W_H + (224 * g + 32 * w) * 2 + n * 4 : OOB; }
  u32 o_dg = w < 7 ? W_D + g * WD_REP + (l >> 3) * 896 + w * 128 + (l & 7) * 16 : OOB;
  u32 o_ag = w < 7 ? W_A + g * WA_REP + (l >> 3) * 896 + w * 128 + (l & 7) * 16 : OOB;
  u32 dcol = w < 7 ? 64 * w + 8 * (l & 7) + 4 * (l >> 5) + 2 * ((l >> 4) & 1) + ((l >> 3) & 1) : 0;
  pin(o_cs); pin(o_ag); pin(o_mg0); pin(o_mg1); pin(o_pub); pin(dcol);
  if (LO) { pin(o_qs0); pin(o_qs1); pin(o_qg0); pin(o_qg1); pin(o_ms0); pin(o_ms1); }
  if (ROLE == 0) pin(o_cg);
  if (ROLE == 2) pin(o_gat);
  if (ROLE != 0) pin(o_dg);
  const int lc = min(l, 55);
  const u32 onem = l < 56 ? ONE : 0u;
  const v4u onem4 = {onem, onem, onem, onem};
  SB();
  u32 T0 = 0, T1 = 0, R0 = 0, R1 = 0, R2 = 0, R3 = 0;
  auto lo_sync = [&](u32 target) {
    asm volatile("s_waitcnt lgkmcnt(0)" ::: "memory");
    if (l == 0) __hip_atomic_fetch_add((LAS u32*)(L + S_CNT), 1u, __ATOMIC_RELAXED, __HIP_MEMORY_SCOPE_WORKGROUP);
    while (*(volatile LAS u32*)(L + S_CNT) < target) {}
    asm volatile("" ::: "memory");
  };
  v4u kf[8];
  v4u wqf[4];
  f4 Sq;
  auto q_phase = [&](const v4u xs) {
    f4 acc = {0.f, 0.f, 0.f, 0.f};
#pragma unroll
    for (int s = 0; s < 4; ++s) acc = mfma(wqf[s], *(LAS v4u*)(L + S_XB + (112 * kq + 32 * s + 8 * u) * 2), acc);
    float mu, r; ln_stats(xs, mu, r);
    return v2u{cvtpk((acc[0] - mu * Sq[0]) * r, (acc[1] - mu * Sq[1]) * r), cvtpk((acc[2] - mu * Sq[2]) * r, (acc[3] - mu * Sq[3]) * r)};
  };
  if constexpr (LO) {
#pragma unroll
    for (int i = 0; i < 4; ++i) *(LAS v4u*)(L + S_WQ + (t + 256 * i) * 16) = wq_raw[i];
    if (w == 0) { *(LAS v4u*)(L + S_XB + l * 16) = xp; if (l == 0) *(LAS v4u*)(L + S_EP) = ep; }
    lo_sync(4);
    STAMP(11);
    xp = *(LAS v4u*)(L + S_XB + l * 16);
    { const v4u e = *(LAS v4u*)(L + S_EP);
      T0 = __builtin_amdgcn_readfirstlane(e.x) + 1u; T1 = __builtin_amdgcn_readfirstlane(e.y); const u32 T2 = __builtin_amdgcn_readfirstlane(e.z);
      R0 = T0; R1 = T0 ^ T1; R2 = T0 ^ T2; R3 = T0 ^ T1 ^ T2 ^ 0x9E3779B9u; }
#pragma unroll
    for (int s = 0; s < 4; ++s) {
      LAS unsigned char* a = L + S_WQ + (32 * s + 8 * u + (n >> 2)) * 128 + w * 32 + (n & 3) * 8;
      const v2u h0 = __builtin_bit_cast(v2u, __builtin_amdgcn_ds_read_tr16_b64_v4i16((LAS s4v*)a));
      const v2u h1 = __builtin_bit_cast(v2u, __builtin_amdgcn_ds_read_tr16_b64_v4i16((LAS s4v*)(a + 512)));
      wqf[s] = v4u{h0.x, h0.y, h1.x, h1.y};
    }
    Sq = f4{0.f, 0.f, 0.f, 0.f};
#pragma unroll
    for (int s = 0; s < 4; ++s) Sq = mfma(wqf[s], v4u{ONE, ONE, ONE, ONE}, Sq);
    { const v2u qv = q_phase(xp);
      asm volatile("s_waitcnt vmcnt(8)" ::: "memory");
      stx4(v4u{qv.x, qv.y, T0, T1}, rws, o_qs0, 0); }
    STAMP(1);
    __builtin_amdgcn_raw_buffer_store_b32(w == 0 ? R0 : w == 1 ? R1 : w == 2 ? R2 : R3, rws, l == 0 ? WS_RDY + c * 16 + w * 4 : OOB, 0, 16);
#pragma unroll
    for (int i = 0; i < 8; ++i) { const int idx = t + 256 * i, key = idx >> 5; *(LAS v4u*)(L + S_K + key * 512 + (((idx & 31) ^ (key & 15)) * 16)) = k_raw[i]; }
    lo_sync(8);
#pragma unroll
    for (int s = 0; s < 8; ++s) kf[s] = *(LAS v4u*)(L + S_K + (16 * w + n) * 512 + (((4 * s + u) ^ n) * 16));
    STAMP(14);
  }

  v4u vf[2][2];
  float S13 = 0.f;
  v4u wx[8];
  u32 w2_raw[32];

  auto load_wx = [&](int i0, int i1) {
    const int tt = t - 256;
    if (ROLE == 0) {
      const rsrc_t rwo = mkrs(p.wo, DM * NQ * 2);
#pragma unroll
      for (int i = 0; i < 8; ++i) if (i >= i0 && i < i1) wx_raw[i] = ldw(rwo, (h * 256 + (tt >> 3) + 32 * i) * (DM * 2) + cb * 128 + (tt & 7) * 16);
    } else if (ROLE == 1) {
      const rsrc_t r1 = mkrs(isup ? p.w3 : p.w1, DM * FF * 2);
      const u32 o = (tt >> 2) * (FF * 2) + ub * 64 + (tt & 3) * 16;
#pragma unroll
      for (int i = 0; i < 7; ++i) if (i >= i0 && i < i1) wx_raw[i] = ldw(r1, o + i * 64 * (FF * 2));
    }
  };

  auto layer = [&](auto LC) {
    constexpr int LY = decltype(LC)::value;
    constexpr u32 SC = LY * WC_LAYER, SD = LY * WD_LAYER, SH = LY * WH_LAYER, SA = LY * WA_LAYER;
    constexpr int SO = 16 * LY;
    const u32 o_ms = LY == 0 ? o_ms0 : o_ms1, o_mg = LY == 0 ? o_mg0 : o_mg1;
    constexpr u32 SM = 0;
    if (LO) {
      v2u qa, qb;
      if (LY == 0) {
        DLY(D_B0);
        wait_until(t_start + DLV(D_TB0));
        auto qbad = [&](const v4u& q) { return (q.z != T0) | (q.w != T1); };
        v4u q0 = ldx4(rws, o_qg0, 0), q1 = ldx4(rws, o_qg0 + 2048, 0);
        SB();
        if (l == 0) __hip_atomic_fetch_add((LAS u32*)(L + S_CNT + 4), 1u, __ATOMIC_RELAXED, __HIP_MEMORY_SCOPE_WORKGROUP);
        SB();
        bool bad = qbad(q0) | qbad(q1);
        REPOLL(32, bad, (q0 = ldx4(rws, o_qg0, 0), q1 = ldx4(rws, o_qg0 + 2048, 0)), qbad(q0) | qbad(q1));
        qa = v2u{q0.x, q0.y}; qb = v2u{q1.x, q1.y};
      } else {
        if (LY == 1) STAMP(50);
        qa = ldx2(rws, o_qg1, 0); qb = ldx2(rws, o_qg1 + 1024, 0);
        bool bad = max(max(qa.x, qa.y), max(qb.x, qb.y)) == SENT;
        REPOLL(40, bad, (qa = ldx2(rws, o_qg1, 0), qb = ldx2(rws, o_qg1 + 1024, 0)), max(max(qa.x, qa.y), max(qb.x, qb.y)) == SENT);
      }
      const float c01 = sw32_add(bflo(qa.x) + bflo(qb.x), bflo(qa.y) + bflo(qb.y));
      const float c13 = sw32_add(bfhi(qa.x) + bfhi(qb.x), bfhi(qa.y) + bfhi(qb.y));
      *(LAS u32*)(L + S_Q + (w >> 1) * 576 + ((32 * w + (l & 31)) & 63) * 8 + (l >> 5) * 4) = cvtpk(c01, c13);
      if (LY == 0) lo_sync(12); else lo_sync(16);
    } else if (LY == 0) {
      if ((ROLE == 0 && WOE > 0) || (ROLE == 1 && WFE > 0)) {
        while (*(volatile LAS u32*)(L + S_CNT + 4) < 4u) __builtin_amdgcn_s_sleep(1);
        asm volatile("" ::: "memory");
        load_wx(0, ROLE == 0 ? WOE : WFE);
      }
      const int tt = t - 256;
      SB();
      STAMP(30);
#pragma unroll
      for (int i = 0; i < 8; ++i) { const int idx = tt + 256 * i, row = idx >> 5, gr = idx & 31; *(LAS v4u*)(L + S_V + row * 512 + ((gr ^ (4 * (row & 3))) * 16)) = v_raw[i]; }
      STAMP(31);
    }
    STAMP(2 + SO);
    if (LO) {
      LAS unsigned char* Q = L + S_Q;
      f4 acc = {0.f, 0.f, 0.f, 0.f};
#pragma unroll
      for (int s = 0; s < 8; ++s) { const v4u qf = *(LAS v4u*)(Q + (n & 1) * 576 + s * 64 + u * 16); acc = mfma(kf[s], qf, acc); }
      if (n < 2) *(LAS f4*)(L + S_SC + n * 272 + (16 * w + 4 * u) * 4) = acc;
    }
    BAR();
    STAMP(3 + SO);
    if (LY == 0) {
#pragma unroll
      for (int tl = 0; tl < 2; ++tl)
#pragma unroll
        for (int st = 0; st < 2; ++st) {
          const int key = 32 * st + 8 * u + (n >> 2), gr = 4 * w + (n & 3);
          LAS unsigned char* a = L + S_V + key * 512 + ((gr ^ (4 * (key & 3))) * 16) + tl * 8;
          const v2u h0 = __builtin_bit_cast(v2u, __builtin_amdgcn_ds_read_tr16_b64_v4i16((LAS s4v*)a));
          const v2u h1 = __builtin_bit_cast(v2u, __builtin_amdgcn_ds_read_tr16_b64_v4i16((LAS s4v*)(a + 2048)));
          vf[tl][st] = v4u{h0.x, h0.y, h1.x, h1.y};
        }
    }
    {
      const float s0 = *(LAS float*)(L + S_SC + l * 4), s1 = *(LAS float*)(L + S_SC + 272 + l * 4);
      constexpr float CE = 0.0625f * 1.4426950408889634f;
      float m = row_max(sw32_max(s0, s1));
      m = sw16_max(m, m) * CE;
      auto ms = __builtin_amdgcn_permlane32_swap(__float_as_uint(m), __float_as_uint(m), false, false);
      const float p0 = __builtin_amdgcn_exp2f(fmaf(s0, CE, -__uint_as_float(ms[0]))), p1 = __builtin_amdgcn_exp2f(fmaf(s1, CE, -__uint_as_float(ms[1])));
      const u32 pk = cvtpk(p0, p1);
      *(LAS u16*)(L + S_P + w * 320 + l * 2) = (u16)pk;
      *(LAS u16*)(L + S_P + w * 320 + 160 + l * 2) = (u16)(pk >> 16);
      const v4u pf0 = *(LAS v4u*)(L + S_P + w * 320 + (n & 1) * 160 + u * 16), pf1 = *(LAS v4u*)(L + S_P + w * 320 + (n & 1) * 160 + 64 + u * 16);
      f4 o0 = {0.f, 0.f, 0.f, 0.f}, o1 = o0;
      o0 = mfma(vf[0][0], pf0, o0); o1 = mfma(vf[1][0], pf0, o1);
      o0 = mfma(vf[0][1], pf1, o0); o1 = mfma(vf[1][1], pf1, o1);
      stx4(v4u{cvtpk(o0[0], o0[1]), cvtpk(o0[2], o0[3]), cvtpk(o1[0], o1[1]), cvtpk(o1[2], o1[3])}, rws, o_cs, SC);
      if (LO) {
        float ls = row_sum(sw32_add(p0, p1)); ls = sw16_add(ls, ls);
        stx2(v2u{__float_as_uint(m), __float_as_uint(ls)}, rws, w == 0 ? o_ms : OOB, SM);
      }
    }
    STAMP(4 + SO);
    if (!(LY == 0 && !LO && ROLE != 0)) DLY(LY == 0 ? D_C0 : D_C1);
    if constexpr (ROLE == 0) {
      float Ls = 1.f;
      if (LY == 0) {
        if (LO) {
          if (w == 0) {
            v2u m0 = ldx2(rws, o_mg, SM);
            bool badm = max(m0.x, m0.y) == SENT;
            REPOLL(38, badm, m0 = ldx2(rws, o_mg, SM), max(m0.x, m0.y) == SENT);
            Ls = split_norm(split_weights(m0, L + S_E, l), m0);
          }
        } else {
          load_wx(WOE, 8);
          SB();
#pragma unroll
          for (int i = 0; i < 8; ++i) *(LAS v4u*)(L + S_WX + (t - 256 + 256 * i) * 16) = wx_raw[i];
        }
        BAR();
        DLY(D_C0B);
      }
      if (LY == 1) STAMP(48);
      v2u ml = {0u, 0u};
      if (LY == 1) ml = ldx2(rws, o_mg, SM);
      v4u a0 = ldx4(rws, o_cg, SC), a1 = ldx4(rws, o_cg + 1024, SC);
      if (LY == 0) {
        SB();
#pragma unroll
        for (int ks = 0; ks < 2; ++ks)
#pragma unroll
          for (int ct = 0; ct < 4; ++ct) {
            LAS unsigned char* a = L + S_WX + (64 * (w >> 1) + 32 * ks + 8 * u + (n >> 2)) * 128 + ct * 32 + (n & 3) * 8;
            const v2u h0 = __builtin_bit_cast(v2u, __builtin_amdgcn_ds_read_tr16_b64_v4i16((LAS s4v*)a));
            const v2u h1 = __builtin_bit_cast(v2u, __builtin_amdgcn_ds_read_tr16_b64_v4i16((LAS s4v*)(a + 512)));
            wx[4 * ks + ct] = v4u{h0.x, h0.y, h1.x, h1.y};
          }
      }
      const float xres = (g == 0 && hh == 0) ? bflo(*(LAS u16*)(L + S_XB + (64 * cb + l) * 2)) : 0.f;
      bool bad = max(max(vmax(a0), vmax(a1)), max(ml.x, ml.y)) == SENT;
      if (LY == 0) REPOLL(34, bad, (a0 = ldx4(rws, o_cg, SC), a1 = ldx4(rws, o_cg + 1024, SC)), max(vmax(a0), vmax(a1)) == SENT);
      else REPOLL(42, bad, (ml = ldx2(rws, o_mg, SM), a0 = ldx4(rws, o_cg, SC), a1 = ldx4(rws, o_cg + 1024, SC)), max(max(vmax(a0), vmax(a1)), max(ml.x, ml.y)) == SENT);
      STAMP(5 + SO);
      f4 d[4];
#pragma unroll
      for (int ct = 0; ct < 4; ++ct) d[ct] = mfma(a0, wx[ct], f4{0.f, 0.f, 0.f, 0.f});
#pragma unroll
      for (int ct = 0; ct < 4; ++ct) d[ct] = mfma(a1, wx[4 + ct], d[ct]);
      LAS unsigned char* E = L + S_E + (LY == 0 ? 0 : w * 128);
      float e1 = 0.f;
      if (LY == 1) e1 = split_weights(ml, E, l);
      const f4 ev = *(LAS f4*)(E + (16 * (w & 1) + 4 * u) * 4);
      if (LY == 1 && LO) Ls = split_norm(e1, ml);
      float cs[4];
#pragma unroll
      for (int ct = 0; ct < 4; ++ct) cs[ct] = ev[0] * d[ct][0] + ev[1] * d[ct][1] + ev[2] * d[ct][2] + ev[3] * d[ct][3];
      const float z = sw16_add(sw32_add(cs[0], cs[2]), sw32_add(cs[1], cs[3]));
      *(LAS float*)(L + S_PART + w * 256 + l * 4) = z;
      BAR();
      if (LO && w == 0) {
        float s = 0.f;
#pragma unroll
        for (int i = 0; i < 8; ++i) s += *(LAS float*)(L + S_PART + i * 256 + l * 4);
        s = fmaf(s, __builtin_amdgcn_rcpf(Ls), xres);
        const u32 pk = cvtpk(s, dpp<QX1>(s));
#pragma unroll
        for (int r = 0; r < 8; ++r) stx1(pk, rws, o_pub, SD + r * WD_REP);
      }
      STAMP(6 + SO);
      if (LY == 1) return;
      RDY_CHECK();
      BAR();
      DLY(D_A1);
    } else {
      u64 t_ml = 0;
      if (LY == 1 || LO) {
        v2u ml = ldx2(rws, o_mg, SM);
        bool bad = max(ml.x, ml.y) == SENT;
        REPOLL(34 + SO / 2, bad, ml = ldx2(rws, o_mg, SM), max(ml.x, ml.y) == SENT);
        t_ml = __builtin_amdgcn_s_memrealtime();
      }
      STAMP(5 + SO);
      if (LY == 0) {
        if (LO && t == 0) *(LAS u64*)(L + S_TML) = t_ml;
        RDY_CHECK();
        if (ROLE == 2) {
          const rsrc_t rw2 = mkrs(p.w2, DM * FF * 2);
          const u32 o = (w < 7 && 128 * wa + 2 * l < DM) ? (224 * g + 32 * w) * (DM * 2) + wa * 256 + l * 4 : OOB;
#pragma unroll
          for (int i = 0; i < 32; ++i) w2_raw[i] = __builtin_amdgcn_raw_buffer_load_b32(rw2, o + i * (DM * 2), 0, NTW);
          SB();
          BAR();
        } else {
          if (!LO) {
            const int tt = t - 256;
            load_wx(WFE, 7);
            SB();
#pragma unroll
            for (int i = 0; i < 7; ++i) { const int idx = tt + 256 * i; *(LAS v4u*)(L + S_WX + ((idx ^ ((idx >> 5) & 7)) * 16)) = wx_raw[i]; }
          }
          BAR();
          v2u d[8];
#pragma unroll
          for (int jj = 0; jj < 8; ++jj) d[jj] = *(LAS v2u*)(L + S_WX + (((32 * lc + 4 * jj + (w >> 1)) ^ (lc & 7)) * 16) + (w & 1) * 8);
#pragma unroll
          for (int q = 0; q < 2; ++q)
#pragma unroll
            for (int i = 0; i < 4; ++i) { wx[2 * q][i] = __builtin_amdgcn_perm(d[2 * i + 1][q], d[2 * i][q], 0x05040100u); wx[2 * q + 1][i] = __builtin_amdgcn_perm(d[2 * i + 1][q], d[2 * i][q], 0x07060302u); }
          S13 = rows4(dot8(wx[0], onem4), dot8(wx[1], onem4), dot8(wx[2], onem4), dot8(wx[3], onem4));
        }
        t_ml = *(LAS u64*)(L + S_TML);
      }
      wait_until(t_ml + DLV(LY == 0 ? D_DS0 : D_DS1));
      if (LY == 1) STAMP(49);
      v4u a = ldx4(rws, o_dg, SD), b = ldx4(rws, o_dg + 8 * 896, SD);
      if (LY == 0 && ROLE == 2) {
#pragma unroll
        for (int i = 0; i < 16; ++i) { wx[i >> 2][i & 3] = __builtin_amdgcn_perm(w2_raw[2 * i + 1], w2_raw[2 * i], 0x05040100u);
                                       wx[4 + (i >> 2)][i & 3] = __builtin_amdgcn_perm(w2_raw[2 * i + 1], w2_raw[2 * i], 0x07060302u); }
      }
      bool bad = max(vmax(a), vmax(b)) == SENT;
      REPOLL(35 + SO / 2, bad, (a = ldx4(rws, o_dg, SD), b = ldx4(rws, o_dg + 8 * 896, SD)), max(vmax(a), vmax(b)) == SENT);
      STAMP(7 + SO);
      float v8[8];
#pragma unroll
      for (int i = 0; i < 4; ++i) { v8[2 * i] = bflo(a[i]) + bflo(b[i]); v8[2 * i + 1] = bfhi(a[i]) + bfhi(b[i]); }
      const float x1 = reduce_rows(v8, l);
      if constexpr (ROLE == 1) {
        if (w < 7) *(LAS u16*)(L + S_XB + dcol * 2) = (u16)cvtpk(x1, x1);
        BAR();
        const v4u xb = *(LAS v4u*)(L + S_XB + l * 16);
        float mu, r; ln_stats(xb, mu, r);
        float y = (rows4(dot8(xb, wx[0]), dot8(xb, wx[1]), dot8(xb, wx[2]), dot8(xb, wx[3])) - mu * S13) * r;
        if (!isup) y *= __builtin_amdgcn_rcpf(1.f + __builtin_amdgcn_exp2f(-1.4426950408889634f * y));
        auto s = __builtin_amdgcn_permlane16_swap(__float_as_uint(y), __float_as_uint(y), false, false);
        const u32 pk = cvtpk(__uint_as_float(s[0]), __uint_as_float(s[1]));
        auto s2 = __builtin_amdgcn_permlane32_swap(pk, pk, false, false);
        stx2(v2u{s2[0], s2[1]}, rws, o_pub, SH);
        STAMP(8 + SO);
        if (LY == 1) return;
        DLY(D_A2);
      } else {
        if (w < 7) *(LAS float*)(L + S_X1 + dcol * 4) = x1;
        DLY(LY == 0 ? D_H0 : D_H1);
        u32 hg = ldx1(rws, o_gat, SH), hu = ldx1(rws, o_gat, SH + WH_UP);
        bool bad2 = max(hg, hu) == SENT;
        REPOLL(36 + SO / 2, bad2, (hg = ldx1(rws, o_gat, SH), hu = ldx1(rws, o_gat, SH + WH_UP)), max(hg, hu) == SENT);
        STAMP(8 + SO);
        const u32 hp = cvtpk(bflo(hg) * bflo(hu), bfhi(hg) * bfhi(hu));
        f2 acc = {0.f, 0.f};
        static_for<16>([&](auto I) { constexpr int i = decltype(I)::value;
          const u32 hb = (u32)__builtin_amdgcn_mov_dpp((int)hp, 0x150 + i, 0xf, 0xf, true);
          acc.x = dot2(hb, wx[i >> 2][i & 3], acc.x); acc.y = dot2(hb, wx[4 + (i >> 2)][i & 3], acc.y); });
        *(LAS f2*)(L + S_PART + w * 512 + l * 8) = acc;
        BAR();
        if (LO && w == 0) {
          f2 s = {0.f, 0.f};
          if (g == 0) s = *(LAS f2*)(L + S_X1 + min(128 * wa + 2 * l, DM - 2) * 4);
#pragma unroll
          for (int i = 0; i < 7; ++i) s += *(LAS f2*)(L + S_PART + i * 512 + l * 8);
          const u32 pk = cvtpk(s.x, s.y);
          if (LY == 0) {
#pragma unroll
            for (int r = 0; r < 8; ++r) stx1(pk, rws, o_pub, SA + r * WA_REP);
          } else {
            stx1(pk, rws, o_pub, SA + 2 * wa * WA_REP); stx1(pk, rws, o_pub, SA + (2 * wa + 1) * WA_REP);
          }
        }
        STAMP(9 + SO);
        if (LY == 1) {
          if (LO && w == 0 && j == 28 && g < 7) {
            DLY(D_F);
            const u32 o = W_A + g * WA_REP + (l >> 3) * 896 + g * 128 + (l & 7) * 16;
            v4u f = ldx4(rws, o, SA);
            bool bad3 = vmax(f) == SENT;
            REPOLL(45, bad3, f = ldx4(rws, o, SA), vmax(f) == SENT);
            float v[8];
#pragma unroll
            for (int i = 0; i < 4; ++i) { v[2 * i] = bflo(f[i]); v[2 * i + 1] = bfhi(f[i]); }
            const float y = reduce_rows(v, l);
            p.out[64 * g + 8 * (l & 7) + 4 * (l >> 5) + 2 * ((l >> 4) & 1) + ((l >> 3) & 1)] = (u16)cvtpk(y, y);
            if (g == 0 && l == 0) stx1(T0, rws, WS_EP, 0);
            STAMP(10 + SO);
          }
          return;
        }
        DLY(D_A3);
      }
    }
    {
      v4u a = ldx4(rws, o_ag, SA);
      bool bad = vmax(a) == SENT;
      REPOLL(37, bad, a = ldx4(rws, o_ag, SA), vmax(a) == SENT);
      STAMP(10 + SO);
      float v8[8];
#pragma unroll
      for (int i = 0; i < 4; ++i) { v8[2 * i] = bflo(a[i]); v8[2 * i + 1] = bfhi(a[i]); }
      const float x2 = reduce_rows(v8, l);
      if (w < 7) *(LAS u16*)(L + S_XB + dcol * 2) = (u16)cvtpk(x2, x2);
      BAR();
      if (LO) {
        v4u xb = *(LAS v4u*)(L + S_XB + l * 16);
        const v2u qv = q_phase(xb);
        stx2(qv, rws, o_qs1, 0);
        STAMP(17);
        DLY(D_B1);
      }
    }
  };
  layer(std::integral_constant<int, 0>{});
  layer(std::integral_constant<int, 1>{});
  STAMP_FLUSH();
  asm volatile("; end of program %0 %1" :: "n"(ROLE), "n"(LO ? 1 : 0));
}

__global__ void __launch_bounds__(NT, 2) mega(Params p) {
  extern __shared__ __attribute__((aligned(16))) unsigned char lds_[];
  LAS unsigned char* L = (LAS unsigned char*)lds_;
  const int c = blockIdx.x, t = threadIdx.x, w = __builtin_amdgcn_readfirstlane(t >> 6), j = c >> 3;
  if (j < 14) { if (w < 4) program<0, true>(p, L, c, t, w); else program<0, false>(p, L, c, t, w); }
  else if (j < 28) { if (w < 4) program<1, true>(p, L, c, t, w); else program<1, false>(p, L, c, t, w); }
  else { if (w < 4) program<2, true>(p, L, c, t, w); else program<2, false>(p, L, c, t, w); }
}
}

extern "C" void kernel_launch(void* const* d_in, const int* in_sizes, int n_in, void* d_out, int out_size, void* d_ws, size_t ws_size, hipStream_t stream) {
  mk::Params p;
  p.x = (const mk::u16*)d_in[0]; p.k = (const mk::u16*)d_in[1]; p.v = (const mk::u16*)d_in[2];
  p.wq = (const mk::u16*)d_in[3]; p.wo = (const mk::u16*)d_in[4];
  p.w1 = (const mk::u16*)d_in[6]; p.w3 = (const mk::u16*)d_in[7]; p.w2 = (const mk::u16*)d_in[8];
  p.out = (mk::u16*)d_out; p.ws = (unsigned char*)d_ws;
  (void)hipFuncSetAttribute((const void*)mk::mega, hipFuncAttributeMaxDynamicSharedMemorySize, mk::LDS_BYTES);
  hipLaunchKernelGGL(mk::mega, dim3(256), dim3(mk::NT), mk::LDS_BYTES, stream, p);
}
```

```cpp
#include <hip/hip_runtime.h>
#include <stdint.h>
#include <type_traits>

namespace mk {
typedef unsigned u32; typedef unsigned short u16; typedef unsigned long long u64;
typedef u32 v4u __attribute__((ext_vector_type(4)));
typedef u32 v2u __attribute__((ext_vector_type(2)));
typedef float f2 __attribute__((ext_vector_type(2)));
typedef float f4 __attribute__((ext_vector_type(4)));
typedef short s4v __attribute__((ext_vector_type(4)));
typedef __bf16 bf2 __attribute__((ext_vector_type(2)));
typedef __bf16 bf8 __attribute__((ext_vector_type(8)));
#define LAS __attribute__((address_space(3)))
#define DEV __device__ __forceinline__

constexpr int DM = 448, SK = 8192, DH = 256, WIN = 2048, FF = 1792, NQ = 4096;
constexpr int NT = 512;
constexpr int WS_EP = 0;
constexpr int WS_RDY = 4096;
constexpr int W_Q0 = 8192, WQ0_GROUP = 8192;
constexpr int W_C0 = W_Q0 + 8 * WQ0_GROUP;
constexpr int W_ML0 = W_C0 + 262144;
constexpr int W_D0 = W_ML0 + 65536, WD0_REP = 16384;
constexpr int W_TWO = (W_D0 + 8 * WD0_REP + 16383) / 16384 * 16384;
__host__ __device__ constexpr int two(int o) { return W_TWO + ((o >> 13) << 14) + (o & 8191); }
constexpr int L_Q1 = 0, LQ1_GROUP = 4096;
constexpr int L_C1 = L_Q1 + 8 * LQ1_GROUP;
constexpr int L_ML1 = L_C1 + 262144;
constexpr int L_D1 = L_ML1 + 8192, LD1_REP = 16384;
constexpr int L_H = L_D1 + 8 * LD1_REP, LH_LAYER = 8192, LH_UP = 4096;
constexpr int L_A = L_H + 2 * LH_LAYER, LA_REP = 8192, LA_LAYER = 8 * LA_REP;
constexpr int L_PR = L_A + 2 * LA_LAYER;
constexpr int WS_BYTES = W_TWO + 2 * (L_PR + 131072);
static_assert(WS_BYTES <= (2 << 20), "one 2-MB page");
constexpr int WS_STAMP = 4 << 20;
constexpr u32 SENT = 0xFFFFFFFFu;
constexpr u32 OOB = 0x80000000u;

constexpr int S_WQ = 0;
constexpr int S_K = S_WQ + 16384;
constexpr int S_V = S_K + 32768;
constexpr int S_WX = S_V + 32768;
constexpr int S_Q = S_WX + 32768;
constexpr int S_SC = S_Q + 1280;
constexpr int S_P = S_SC + 640;
constexpr int S_E = S_P + 8 * 320;
constexpr int S_PART = S_E + 8 * 128;
constexpr int S_XB = S_PART + 4096;
constexpr int S_X1 = S_XB + 1024;
constexpr int S_HB = S_X1 + 1792;
constexpr int S_CNT = S_HB + 32;
constexpr int S_EP = S_HB + 48;
constexpr int S_TML = S_HB + 64;
constexpr int S_END = S_HB + 80;
constexpr int LDS_BYTES = (S_END + 255) / 256 * 256;
static_assert(LDS_BYTES > 81920 && LDS_BYTES <= 163840, "LDS must pin one workgroup per CU");

struct Params {
  const u16 *x, *k, *v, *wq, *wo, *w1, *w3, *w2;
  u16* out;
  unsigned char* ws;
};

#define SPIN_CAP (1u << 18)
#define BACKOFF 1
#define NTW 2
#define WARM 1
#define WARM_AUX 2
enum { D_B0, D_TB0, D_B1, D_C0, D_C1, D_DS0, D_DS1, D_H0, D_H1, D_A1, D_A2, D_A3, D_F, D_C0B, D_DSH, D_N };
#define DL_DEFAULT {20, 340, 18, 24, 18, 60, 62, 54, 48, 110, 60, 12, 8, 2, 30}
#define PROBE_THR 110
#define WOE 8
#define WFE 4
#define DLV(k) dl_default[k]
constexpr int dl_default[D_N] = DL_DEFAULT;

DEV float bflo(u32 v) { return __uint_as_float(v << 16); }
DEV u32 cvtpk(float a, float b) { f2 v = {a, b}; bf2 r = __builtin_convertvector(v, bf2); return __builtin_bit_cast(u32, r); }
DEV float dot2(u32 a, u32 b, float c) { return __builtin_amdgcn_fdot2_f32_bf16(__builtin_bit_cast(bf2, a), __builtin_bit_cast(bf2, b), c, false); }
DEV f4 mfma(v4u a, v4u b, f4 c) { return __builtin_amdgcn_mfma_f32_16x16x32_bf16(__builtin_bit_cast(bf8, a), __builtin_bit_cast(bf8, b), c, 0, 0, 0); }
template <int C> DEV float dpp(float x) { return __builtin_bit_cast(float, __builtin_amdgcn_mov_dpp(__builtin_bit_cast(int, x), C, 0xf, 0xf, true)); }
constexpr int ROR8 = 0x128, ROR4 = 0x124, ROR2 = 0x122, ROR1 = 0x121, QX1 = 0xB1, QX2 = 0x4E, HMIR = 0x141;
DEV float sw32_add(float a, float b) { auto s = __builtin_amdgcn_permlane32_swap(__float_as_uint(a), __float_as_uint(b), false, false); return __uint_as_float(s[0]) + __uint_as_float(s[1]); }
DEV float sw16_add(float a, float b) { auto s = __builtin_amdgcn_permlane16_swap(__float_as_uint(a), __float_as_uint(b), false, false); return __uint_as_float(s[0]) + __uint_as_float(s[1]); }
DEV float vmaxf(float a, float b) { return __builtin_amdgcn_fmed3f(a, b, __builtin_inff()); }
DEV float sw32_max(float a, float b) { auto s = __builtin_amdgcn_permlane32_swap(__float_as_uint(a), __float_as_uint(b), false, false); return vmaxf(__uint_as_float(s[0]), __uint_as_float(s[1])); }
DEV float sw16_max(float a, float b) { auto s = __builtin_amdgcn_permlane16_swap(__float_as_uint(a), __float_as_uint(b), false, false); return vmaxf(__uint_as_float(s[0]), __uint_as_float(s[1])); }
DEV float row_sum(float z) { z += dpp<ROR8>(z); z += dpp<ROR4>(z); z += dpp<ROR2>(z); z += dpp<ROR1>(z); return z; }
DEV float row_max(float z) { z = vmaxf(z, dpp<ROR8>(z)); z = vmaxf(z, dpp<ROR4>(z)); z = vmaxf(z, dpp<ROR2>(z)); z = vmaxf(z, dpp<ROR1>(z)); return z; }
DEV float sum4(float a, float b, float c, float d) { return row_sum(sw16_add(sw32_add(a, c), sw32_add(b, d))); }
DEV float rdl(float v, int l) { return __uint_as_float(__builtin_amdgcn_readlane(__float_as_uint(v), l)); }
DEV void pin(u32& v) { asm volatile("" : "+v"(v)); }
DEV void pin(v4u& v) { asm volatile("" : "+v"(v)); }
#define SB() __builtin_amdgcn_sched_barrier(0)
#define BAR() do { asm volatile("s_waitcnt lgkmcnt(0)" ::: "memory"); __builtin_amdgcn_s_barrier(); asm volatile("" ::: "memory"); } while (0)
DEV void wait_until(u64 t) { asm volatile("" ::: "memory"); while (__builtin_amdgcn_s_memrealtime() < t) {} asm volatile("" ::: "memory"); SB(); }
#define DLY(k) wait_until(__builtin_amdgcn_s_memrealtime() + DLV(k))
#define CNT(i)
#define STAMP(i)
#define STAMP_FLUSH()
#define REPOLL(id, bad, LOADS, BAD) do { if (__builtin_expect(__any(bad), 0)) { for (u32 sp_ = 0; sp_ < SPIN_CAP; ++sp_) { asm volatile("" ::: "memory"); CNT(id); \
    if (BACKOFF) __builtin_amdgcn_s_sleep(BACKOFF); LOADS; if (!__any(BAD)) break; } } asm volatile("" ::: "memory"); } while (0)

typedef __amdgpu_buffer_rsrc_t rsrc_t;
DEV rsrc_t mkrs(const void* p, u32 bytes) { return __builtin_amdgcn_make_buffer_rsrc((void*)p, 0, bytes, 0x00020000); }
DEV v4u ldw(rsrc_t r, u32 off) { return __builtin_amdgcn_raw_buffer_load_b128(r, off, 0, NTW); }
DEV v4u ldx4(rsrc_t r, u32 off, u32 so) { return __builtin_amdgcn_raw_buffer_load_b128(r, off, so, 16); }
DEV v2u ldx2(rsrc_t r, u32 off, u32 so) { return __builtin_amdgcn_raw_buffer_load_b64(r, off, so, 16); }
DEV u32 ldx1(rsrc_t r, u32 off, u32 so) { return __builtin_amdgcn_raw_buffer_load_b32(r, off, so, 16); }
DEV void stx4(v4u v, rsrc_t r, u32 off, u32 so) { __builtin_amdgcn_raw_buffer_store_b128(v, r, off, so, 16); }
DEV void stx2(v2u v, rsrc_t r, u32 off, u32 so) { __builtin_amdgcn_raw_buffer_store_b64(v, r, off, so, 16); }
DEV void stx1(u32 v, rsrc_t r, u32 off, u32 so) { __builtin_amdgcn_raw_buffer_store_b32(v, r, off, so, 16); }
DEV u32 vmax(v4u v) { return max(max(max(v.x, v.y), v.z), v.w); }
constexpr u32 ONE = 0x3F803F80u;

DEV void read_cols(LAS unsigned char* base, int lc, int w, v4u& wa, v4u& wb) {
  u32 d[8];
#pragma unroll
  for (int jj = 0; jj < 8; ++jj) d[jj] = *(LAS u32*)(base + (((16 * lc + 2 * jj + (w >> 2)) ^ (lc & 7)) * 16) + (w & 3) * 4);
#pragma unroll
  for (int i = 0; i < 4; ++i) { wa[i] = __builtin_amdgcn_perm(d[2 * i + 1], d[2 * i], 0x05040100u); wb[i] = __builtin_amdgcn_perm(d[2 * i + 1], d[2 * i], 0x07060302u); }
}
DEV float dot8(const v4u x, const v4u wv) { float a = 0.f;
#pragma unroll
  for (int i = 0; i < 4; ++i) a = dot2(x[i], wv[i], a);
  return a; }
DEV float rows4(float a, float b, float c, float d) { return row_sum(sw16_add(sw32_add(a, c), sw32_add(b, d))); }
DEV void ln_stats(const v4u x, float& mu, float& r) {
  float st = sw32_add(dot8(x, v4u{ONE, ONE, ONE, ONE}), dot8(x, x)); st = row_sum(sw16_add(st, st));
  mu = rdl(st, 0) * (1.f / DM);
  r = __builtin_amdgcn_rsqf(fmaxf(fmaf(rdl(st, 32), 1.f / DM, -mu * mu), 0.f) + 1e-5f);
}
DEV float reduce_rows(const float (&v8)[8], int l) {
  float v4_[4], v2_[2];
#pragma unroll
  for (int i = 0; i < 4; ++i) v4_[i] = sw32_add(v8[i], v8[i + 4]);
#pragma unroll
  for (int i = 0; i < 2; ++i) v2_[i] = sw16_add(v4_[i], v4_[i + 2]);
  const bool k8 = l & 8;
  return (k8 ? v2_[1] : v2_[0]) + dpp<ROR8>(k8 ? v2_[0] : v2_[1]);
}
template <int N, int I = 0, class F> DEV void static_for(F&& f) { if constexpr (I < N) { f(std::integral_constant<int, I>{}); static_for<N, I + 1>(f); } }
DEV float split_weights(const v2u ml, LAS unsigned char* E, int l) {
  const float mv = __uint_as_float(ml.x);
  float M = row_max(mv); M = sw16_max(M, M);
  const float e = __builtin_amdgcn_exp2f(mv - M);
  *(LAS float*)(E + (l & 31) * 4) = e;
  return e;
}
DEV float split_norm(float e, const v2u ml) { float Ls = row_sum(e * __uint_as_float(ml.y)); return sw16_add(Ls, Ls); }
DEV v4u sel_matrix(int n) { v4u a = {0u, 0u, 0u, 0u};
#pragma unroll
  for (int i = 0; i < 4; ++i) a[i] = (n >> 1) == i ? ((n & 1) ? 0x3F800000u : 0x00003F80u) : 0u;
  return a; }
DEV float bfhi(u32 v) { return __uint_as_float(v & 0xFFFF0000u); }

template <int ROLE, bool LO>
DEV void program(const Params& p, LAS unsigned char* L, const int c, const int t, const int w) {
  const int g = c & 7, j = c >> 3, l = t & 63, u = l >> 4, n = l & 15;
  asm volatile("; start of program %0 %1" :: "n"(ROLE), "n"(LO ? 1 : 0) : "memory");
  STAMP(0);
  const u64 t_start = LO ? __builtin_amdgcn_s_memrealtime() : 0;
  const rsrc_t rws = mkrs(p.ws, WS_BYTES);
  const int hh = j >= 7, h = 2 * g + hh, cb = j - 7 * hh;
  const bool isup = j >= 21;
  const int ub = 7 * g + (j - (isup ? 21 : 14));
  const int wa = j - 28;
  const int kq = j >> 3, cq = j & 7;
  const u32 kvbase = ((u32)g * SK + (SK - WIN) + j * 64) * (DH * 2);

  v4u xp = {0, 0, 0, 0}, ep = {0, 0, 0, 0}, wq_raw[4];
  if constexpr (LO) {
    const rsrc_t rwq = mkrs(p.wq, DM * NQ * 2);
    const u32 o = (112 * kq + (t >> 3)) * (NQ * 2) + (g * 512 + cq * 64) * 2 + (t & 7) * 16;
#pragma unroll
    for (int i = 0; i < 4; ++i) wq_raw[i] = ldw(rwq, (i < 3 || t < 128) ? o + i * 32 * (NQ * 2) : OOB);
    xp = __builtin_amdgcn_raw_buffer_load_b128(mkrs(p.x, DM * 2), w == 0 ? l * 16 : OOB, 0, 0);
    ep = ldx4(rws, (w == 0 && l == 0) ? WS_EP : OOB, 0);
    SB();
    u32 oc = OOB;
    if (w == 0) oc = W_C0 + g * 32768 + j * 1024 + l * 16;
    if (w == 1 && ROLE == 0) oc = W_D0 + (l >> 3) * WD0_REP + h * 896 + cb * 128 + (l & 7) * 16;
    if (w == 2 && l < 8) oc = two(L_PR + c * 512 + (l >> 1) * 128) + (l & 1) * 8192;
    if (w == 3 && l < 2) oc = W_ML0 + ((2 * g + l) * 32 + j) * 128;
    stx4(v4u{SENT, SENT, SENT, SENT}, rws, oc, 0);
    if (ROLE != 0) {
      oc = OOB;
      if (ROLE == 1 && w == 0 && l < 8) oc = two(L_H + (isup ? LH_UP : 0) + ub * 64 + (l & 3) * 16) + (l >> 2) * 8192;
      if (ROLE == 2 && (l & 15) + 16 * wa < 56) oc = two(L_A + (2 * w + (l >> 5)) * LA_REP + g * 1024 + wa * 256 + (l & 15) * 16) + ((l >> 4) & 1) * 8192;
      stx4(v4u{SENT, SENT, SENT, SENT}, rws, oc, 0);
    }
    if (t == 0) *(LAS v4u*)(L + S_CNT) = v4u{0u, 0u, 0u, 0u};
    asm volatile("s_waitcnt lgkmcnt(0)" ::: "memory");
  }
  SB();
  __builtin_amdgcn_s_barrier();
  SB();
  STAMP(12);
  v4u k_raw[8], v_raw[8], wx_raw[8];
  if constexpr (LO) {
    const rsrc_t rk = mkrs(p.k, 8u * SK * DH * 2);
#pragma unroll
    for (int i = 0; i < 8; ++i) k_raw[i] = ldw(rk, kvbase + (t + 256 * i) * 16);
  } else {
    const int tt = t - 256;
    const rsrc_t rv = mkrs(p.v, 8u * SK * DH * 2);
#pragma unroll
    for (int i = 0; i < 8; ++i) v_raw[i] = ldw(rv, kvbase + (tt + 256 * i) * 16);
    STAMP(29);
  }
  SB();
  STAMP(13);
  u32 o_qs0 = n == 0 ? W_Q0 + g * WQ0_GROUP + kq * 2048 + (16 * cq + 4 * w + u) * 16 : OOB;
  u32 o_qs1 = n == 0 ? two(L_Q1 + g * LQ1_GROUP + kq * 1024 + (64 * cq + 16 * w + 4 * u) * 2) : OOB;
  u32 o_qg0 = W_Q0 + g * WQ0_GROUP + (l >> 5) * 4096 + (32 * w + (l & 31)) * 16;
  u32 o_qg1 = two(L_Q1 + g * LQ1_GROUP + u * 1024 + (128 * w + 8 * n) * 2);
  const int cofs = (2 * g + n) * 16384 + (j >> 4) * 8192 + w * 1024 + (u * 16 + (j & 15)) * 16;
  u32 o_cs0 = n < 2 ? W_C0 + cofs : OOB, o_cs1 = n < 2 ? two(L_C1 + cofs) : OOB;
  u32 o_ms0 = (l & 31) == 0 ? W_ML0 + ((2 * g + (l >> 5)) * 32 + j) * 128 : OOB, o_ms1 = (l & 31) == 0 ? two(L_ML1 + ((2 * g + (l >> 5)) * 32 + j) * 8) : OOB;
  const int hx = ROLE == 0 ? h : 2 * g;
  const int gofs = hx * 16384 + (w & 1) * 8192 + (w >> 1) * 2048 + l * 16;
  u32 o_cg0 = W_C0 + gofs, o_cg1 = two(L_C1 + gofs);
  u32 o_mg0 = W_ML0 + (hx * 32 + (l & 31)) * 128, o_mg1 = two(L_ML1 + (hx * 32 + (l & 31)) * 8);
  u32 o_pub0 = OOB, o_pub1 = OOB, o_gat = OOB;
  if (ROLE == 0) { o_pub0 = (l & 1) == 0 ? W_D0 + h * 896 + cb * 128 + (l >> 1) * 4 : OOB;
                   o_pub1 = (l & 1) == 0 ? two(L_D1 + h * 1024 + cb * 128 + (l >> 1) * 4) : OOB; }
  else if (ROLE == 1) o_pub0 = l == 0 ? two(L_H + (isup ? LH_UP : 0) + ub * 64 + w * 8) : OOB;
  else { o_pub0 = 128 * wa + 2 * l < DM ? two(L_A + g * 1024 + wa * 256 + l * 4) : OOB;
         o_gat = w < 7 ? two(L_H + (224 * g + 32 * w) * 2 + n * 4) : OOB; }
  const bool cin = LO && 128 * w + 8 * n < DM;
  u32 o_dg0 = cin ? W_D0 + g * WD0_REP + u * 896 + w * 256 + n * 16 : OOB;
  u32 o_dg1 = cin ? two(L_D1 + g * LD1_REP + u * 1024 + w * 256 + n * 16) : OOB;
  u32 o_ag = cin ? two(L_A + g * LA_REP + u * 1024 + w * 256 + n * 16) : OOB;
  u32 a_xw = (cin && u < 2) ? (128 * w + 8 * n + 4 * u) : 0xFFFF;
  v4u selm = sel_matrix(n);
  pin(o_cs0); pin(o_cs1); pin(o_mg0); pin(o_mg1); pin(o_pub0);
  if (LO) { pin(o_ag); pin(a_xw); pin(selm); pin(o_qs0); pin(o_qs1); pin(o_qg0); pin(o_qg1); pin(o_ms0); pin(o_ms1); }
  if (ROLE == 0) { pin(o_cg0); pin(o_cg1); pin(o_pub1); }
  if (ROLE == 2) pin(o_gat);
  if (ROLE != 0 && LO) { pin(o_dg0); pin(o_dg1); }
  const int lc = min(l, 55);
  const u32 onem = l < 56 ? ONE : 0u;
  const v4u onem4 = {onem, onem, onem, onem};
  SB();
  u32 T0 = 0, T1 = 0, R0 = 0, R1 = 0, R2 = 0, R3 = 0;
  auto lo_sync = [&](u32 target) {
    asm volatile("s_waitcnt lgkmcnt(0)" ::: "memory");
    if (l == 0) __hip_atomic_fetch_add((LAS u32*)(L + S_CNT), 1u, __ATOMIC_RELAXED, __HIP_MEMORY_SCOPE_WORKGROUP);
    while (*(volatile LAS u32*)(L + S_CNT) < target) {}
    asm volatile("" ::: "memory");
  };
  v4u kf[8];
  v4u wqf[4];
  f4 Sq;
  auto q_phase = [&](const v4u xs) {
    f4 acc = {0.f, 0.f, 0.f, 0.f};
#pragma unroll
    for (int s = 0; s < 4; ++s) acc = mfma(wqf[s], *(LAS v4u*)(L + S_XB + (112 * kq + 32 * s + 8 * u) * 2), acc);
    float mu, r; ln_stats(xs, mu, r);
    return v2u{cvtpk((acc[0] - mu * Sq[0]) * r, (acc[1] - mu * Sq[1]) * r), cvtpk((acc[2] - mu * Sq[2]) * r, (acc[3] - mu * Sq[3]) * r)};
  };
  if constexpr (LO) {
#pragma unroll
    for (int i = 0; i < 4; ++i) *(LAS v4u*)(L + S_WQ + (t + 256 * i) * 16) = wq_raw[i];
    if (w == 0) { *(LAS v4u*)(L + S_XB + l * 16) = xp; if (l == 0) *(LAS v4u*)(L + S_EP) = ep; }
    lo_sync(4);
    STAMP(11);
    xp = *(LAS v4u*)(L + S_XB + l * 16);
    { const v4u e = *(LAS v4u*)(L + S_EP);
      T0 = __builtin_amdgcn_readfirstlane(e.x) + 1u; T1 = __builtin_amdgcn_readfirstlane(e.y); const u32 T2 = __builtin_amdgcn_readfirstlane(e.z);
      R0 = T0 << 3; R1 = (T0 * 0x9E3779B1u ^ T1) & ~7u; R2 = (T0 * 0x85EBCA77u ^ T2) & ~7u; R3 = (T0 * 0xC2B2AE3Du ^ T1 ^ (T2 >> 7)) & ~7u; }
#pragma unroll
    for (int s = 0; s < 4; ++s) {
      LAS unsigned char* a = L + S_WQ + (32 * s + 8 * u + (n >> 2)) * 128 + w * 32 + (n & 3) * 8;
      const v2u h0 = __builtin_bit_cast(v2u, __builtin_amdgcn_ds_read_tr16_b64_v4i16((LAS s4v*)a));
      const v2u h1 = __builtin_bit_cast(v2u, __builtin_amdgcn_ds_read_tr16_b64_v4i16((LAS s4v*)(a + 512)));
      wqf[s] = v4u{h0.x, h0.y, h1.x, h1.y};
    }
    Sq = f4{0.f, 0.f, 0.f, 0.f};
#pragma unroll
    for (int s = 0; s < 4; ++s) Sq = mfma(wqf[s], v4u{ONE, ONE, ONE, ONE}, Sq);
    { const v2u qv = q_phase(xp);
      asm volatile("s_waitcnt vmcnt(8)" ::: "memory");
      stx4(v4u{qv.x, qv.y, T0, T1}, rws, o_qs0, 0); }
    STAMP(1);
#pragma unroll
    for (int i = 0; i < 8; ++i) { const int idx = t + 256 * i, key = idx >> 5; *(LAS v4u*)(L + S_K + key * 512 + (((idx & 31) ^ (key & 15)) * 16)) = k_raw[i]; }
    lo_sync(8);
#pragma unroll
    for (int s = 0; s < 8; ++s) kf[s] = *(LAS v4u*)(L + S_K + (16 * w + n) * 512 + (((4 * s + u) ^ n) * 16));
    {
      const u32 first = (j ^ w) & 1, oa = two(L_PR + c * 512 + w * 128) + first * 8192, ob = two(L_PR + c * 512 + w * 128) + (first ^ 1) * 8192;
      u32 va = ldx1(rws, l == 0 ? oa : OOB, 0), vb = ldx1(rws, l == 0 ? ob : OOB, 0);
      SB();
      asm volatile("s_waitcnt vmcnt(1)" : "+v"(va) :: "memory");
      const u64 t1 = __builtin_amdgcn_s_memtime();
      asm volatile("s_waitcnt lgkmcnt(0)\n\ts_waitcnt vmcnt(0)" : "+v"(vb) :: "memory");
      const u64 t2 = __builtin_amdgcn_s_memtime();

      u32 xcc; asm volatile("s_getreg_b32 %0, hwreg(HW_REG_XCC_ID, 0, 4)" : "=s"(xcc));
      const u32 die = (xcc >> 2) & 1;
      const u32 vt = (u32)(t2 - t1) > PROBE_THR ? ((first ^ die) ? 1u : 2u) : 0u;
      __builtin_amdgcn_raw_buffer_store_b32((w == 0 ? R0 : w == 1 ? R1 : w == 2 ? R2 : R3) | (die << 2) | vt, rws, l == 0 ? WS_RDY + c * 16 + w * 4 : OOB, 0, 16);
    }
    STAMP(14);
  }

  u32 sg = 0, srep[8];
  auto set_sides = [&]() {
    const v4u cw = *(LAS v4u*)(L + S_CNT);
    const u32 sm = __builtin_amdgcn_readfirstlane(((int)cw.z > 0 ? ~cw.w : cw.w) & 0xFFu);
    sg = ((sm >> g) & 1u) << 13;
#pragma unroll
    for (int r = 0; r < 8; ++r) srep[r] = r * (ROLE == 0 ? 2 * LD1_REP : 2 * LA_REP) + (((sm >> r) & 1u) << 13);
  };
  auto vote = [&]() {
    v4u rdy = ldx4(rws, WS_RDY + t * 16, 0);
    auto rbad = [&]() { return ((rdy.x & ~7u) != R0) | ((rdy.y & ~7u) != R1) | ((rdy.z & ~7u) != R2) | ((rdy.w & ~7u) != R3); };
    bool bad = rbad();
    REPOLL(33, bad, rdy = ldx4(rws, WS_RDY + t * 16, 0), rbad());
    const int votes = __popcll(__ballot(rdy.x & 1u)) + __popcll(__ballot(rdy.y & 1u)) + __popcll(__ballot(rdy.z & 1u)) + __popcll(__ballot(rdy.w & 1u))
                    - __popcll(__ballot(rdy.x & 2u)) - __popcll(__ballot(rdy.y & 2u)) - __popcll(__ballot(rdy.z & 2u)) - __popcll(__ballot(rdy.w & 2u));
    const u64 dies = __ballot(rdy.w & 4u);
    if (l == 0) { __hip_atomic_fetch_add((LAS u32*)(L + S_CNT + 8), (u32)votes, __ATOMIC_RELAXED, __HIP_MEMORY_SCOPE_WORKGROUP);
                  if (w == 0) *(LAS u32*)(L + S_CNT + 12) = (u32)dies & 0xFFu; }
    lo_sync(16);
    set_sides();
    if (ROLE != 0) {
      const int m = j - 14, ch = 2 * m + w;
      u32 oc = OOB;
      if (w < 2) oc = two(ch < 32 ? L_C1 + g * 32768 + ch * 1024 + l * 16 : L_Q1 + g * LQ1_GROUP + (ch - 32) * 1024 + l * 16);
      else if (w == 2) {
        if (m < 16) oc = two(L_D1 + g * LD1_REP + m * 1024 + l * 16);
        else if (m == 16) { if (l < 32) oc = two(L_ML1 + g * 512 + l * 16); }
        else if ((l & 31) < 28) oc = two(L_H + LH_LAYER + (l >> 5) * LH_UP + g * 448 + (l & 31) * 16);
      } else if (m < 8) oc = two(L_A + LA_LAYER + g * LA_REP + m * 1024 + l * 16);
      stx4(v4u{SENT, SENT, SENT, SENT}, rws, oc == OOB ? OOB : oc + sg, 0);
    }
  };

  v4u vf[2][2];
  float S13 = 0.f;
  v4u wx[8];
  u32 w2_raw[32];

  auto load_wx = [&](int i0, int i1) {
    const int tt = t - 256;
    if (ROLE == 0) {
      const rsrc_t rwo = mkrs(p.wo, DM * NQ * 2);
#pragma unroll
      for (int i = 0; i < 8; ++i) if (i >= i0 && i < i1) wx_raw[i] = ldw(rwo, (h * 256 + (tt >> 3) + 32 * i) * (DM * 2) + cb * 128 + (tt & 7) * 16);
    } else if (ROLE == 1) {
      const rsrc_t r1 = mkrs(isup ? p.w3 : p.w1, DM * FF * 2);
      const u32 o = (tt >> 2) * (FF * 2) + ub * 64 + (tt & 3) * 16;
#pragma unroll
      for (int i = 0; i < 7; ++i) if (i >= i0 && i < i1) wx_raw[i] = ldw(r1, o + i * 64 * (FF * 2));
    }
  };

  auto layer = [&](auto LC) {
    constexpr int LY = decltype(LC)::value;
    constexpr u32 SH = LY * 2 * LH_LAYER, SA = LY * 2 * LA_LAYER;
    constexpr int SO = 16 * LY;
    const u32 o_ms = LY == 0 ? o_ms0 : o_ms1, o_mg = LY == 0 ? o_mg0 : o_mg1, o_cs = LY == 0 ? o_cs0 : o_cs1, o_cg = LY == 0 ? o_cg0 : o_cg1, o_dg = LY == 0 ? o_dg0 : o_dg1;
    const u32 SM = LY == 0 ? 0u : sg, SC = SM;
    if (LO) {
      if (LY == 0) {
        DLY(D_B0);
        wait_until(t_start + DLV(D_TB0));
        auto qbad = [&](const v4u& q) { return (q.z != T0) | (q.w != T1); };
        v4u q0 = ldx4(rws, o_qg0, 0), q1 = ldx4(rws, o_qg0 + 2048, 0);
        SB();
        if (l == 0) __hip_atomic_fetch_add((LAS u32*)(L + S_CNT + 4), 1u, __ATOMIC_RELAXED, __HIP_MEMORY_SCOPE_WORKGROUP);
        SB();
        bool bad = qbad(q0) | qbad(q1);
        REPOLL(32, bad, (q0 = ldx4(rws, o_qg0, 0), q1 = ldx4(rws, o_qg0 + 2048, 0)), qbad(q0) | qbad(q1));
        const float c01 = sw32_add(bflo(q0.x) + bflo(q1.x), bflo(q0.y) + bflo(q1.y));
        const float c13 = sw32_add(bfhi(q0.x) + bfhi(q1.x), bfhi(q0.y) + bfhi(q1.y));
        *(LAS u32*)(L + S_Q + (w >> 1) * 576 + ((32 * w + (l & 31)) & 63) * 8 + (l >> 5) * 4) = cvtpk(c01, c13);
        lo_sync(12);
      } else {
        if (LY == 1) STAMP(50);
        v4u q0 = ldx4(rws, o_qg1, sg);
        bool bad = vmax(q0) == SENT;
        REPOLL(40, bad, q0 = ldx4(rws, o_qg1, sg), vmax(q0) == SENT);
        const f4 s = mfma(selm, q0, f4{0.f, 0.f, 0.f, 0.f});
        if (u < 2) *(LAS v2u*)(L + S_Q + (w >> 1) * 576 + ((128 * w + 8 * n + 4 * u) & 255) * 2) = v2u{cvtpk(s[0], s[1]), cvtpk(s[2], s[3])};
      }
    } else if (LY == 0) {
      if ((ROLE == 0 && WOE > 0) || (ROLE == 1 && WFE > 0)) {
        while (*(volatile LAS u32*)(L + S_CNT + 4) < 4u) __builtin_amdgcn_s_sleep(1);
        asm volatile("" ::: "memory");
        load_wx(0, ROLE == 0 ? WOE : WFE);
      }
      const int tt = t - 256;
      SB();
      STAMP(30);
#pragma unroll
      for (int i = 0; i < 8; ++i) { const int idx = tt + 256 * i, row = idx >> 5, gr = idx & 31; *(LAS v4u*)(L + S_V + row * 512 + ((gr ^ (4 * (row & 3))) * 16)) = v_raw[i]; }
      STAMP(31);
    }
    if (LY == 1) BAR();
    STAMP(2 + SO);
    if (LO) {
      LAS unsigned char* Q = L + S_Q;
      f4 acc = {0.f, 0.f, 0.f, 0.f};
#pragma unroll
      for (int s = 0; s < 8; ++s) { const v4u qf = *(LAS v4u*)(Q + (n & 1) * 576 + s * 64 + u * 16); acc = mfma(kf[s], qf, acc); }
      if (n < 2) *(LAS f4*)(L + S_SC + n * 272 + (16 * w + 4 * u) * 4) = acc;
    }
    BAR();
    STAMP(3 + SO);
    if (LY == 0) {
#pragma unroll
      for (int tl = 0; tl < 2; ++tl)
#pragma unroll
        for (int st = 0; st < 2; ++st) {
          const int key = 32 * st + 8 * u + (n >> 2), gr = 4 * w + (n & 3);
          LAS unsigned char* a = L + S_V + key * 512 + ((gr ^ (4 * (key & 3))) * 16) + tl * 8;
          const v2u h0 = __builtin_bit_cast(v2u, __builtin_amdgcn_ds_read_tr16_b64_v4i16((LAS s4v*)a));
          const v2u h1 = __builtin_bit_cast(v2u, __builtin_amdgcn_ds_read_tr16_b64_v4i16((LAS s4v*)(a + 2048)));
          vf[tl][st] = v4u{h0.x, h0.y, h1.x, h1.y};
        }
    }
    {
      const float s0 = *(LAS float*)(L + S_SC + l * 4), s1 = *(LAS float*)(L + S_SC + 272 + l * 4);
      constexpr float CE = 0.0625f * 1.4426950408889634f;
      float m = row_max(sw32_max(s0, s1));
      m = sw16_max(m, m) * CE;
      auto ms = __builtin_amdgcn_permlane32_swap(__float_as_uint(m), __float_as_uint(m), false, false);
      const float p0 = __builtin_amdgcn_exp2f(fmaf(s0, CE, -__uint_as_float(ms[0]))), p1 = __builtin_amdgcn_exp2f(fmaf(s1, CE, -__uint_as_float(ms[1])));
      const u32 pk = cvtpk(p0, p1);
      *(LAS u16*)(L + S_P + w * 320 + l * 2) = (u16)pk;
      *(LAS u16*)(L + S_P + w * 320 + 160 + l * 2) = (u16)(pk >> 16);
      const v4u pf0 = *(LAS v4u*)(L + S_P + w * 320 + (n & 1) * 160 + u * 16), pf1 = *(LAS v4u*)(L + S_P + w * 320 + (n & 1) * 160 + 64 + u * 16);
      f4 o0 = {0.f, 0.f, 0.f, 0.f}, o1 = o0;
      o0 = mfma(vf[0][0], pf0, o0); o1 = mfma(vf[1][0], pf0, o1);
      o0 = mfma(vf[0][1], pf1, o0); o1 = mfma(vf[1][1], pf1, o1);
      stx4(v4u{cvtpk(o0[0], o0[1]), cvtpk(o0[2], o0[3]), cvtpk(o1[0], o1[1]), cvtpk(o1[2], o1[3])}, rws, o_cs, SC);
      if (LO) {
        float ls = row_sum(sw32_add(p0, p1)); ls = sw16_add(ls, ls);
        stx2(v2u{__float_as_uint(m), __float_as_uint(ls)}, rws, w == 0 ? o_ms : OOB, SM);
      }
    }
    STAMP(4 + SO);
    if (!(LY == 0 && !LO && ROLE != 0)) DLY(LY == 0 ? D_C0 : D_C1);
    if constexpr (ROLE == 0) {
      float Ls = 1.f;
      if (LY == 0) {
        if (LO) {
          if (w == 0) {
            v2u m0 = ldx2(rws, o_mg, SM);
            bool badm = max(m0.x, m0.y) == SENT;
            REPOLL(38, badm, m0 = ldx2(rws, o_mg, SM), max(m0.x, m0.y) == SENT);
            Ls = split_norm(split_weights(m0, L + S_E, l), m0);
          }
          STAMP(53);
        } else {
          load_wx(WOE, 8);
          SB();
#pragma unroll
          for (int i = 0; i < 8; ++i) *(LAS v4u*)(L + S_WX + (t - 256 + 256 * i) * 16) = wx_raw[i];
        }
        BAR();
        STAMP(54);
        DLY(D_C0B);
      }
      if (LY == 1) STAMP(48);
      v2u ml = {0u, 0u};
      if (LY == 1) ml = ldx2(rws, o_mg, SM);
      v4u a0 = ldx4(rws, o_cg, SC), a1 = ldx4(rws, o_cg + 1024, SC);
      if (LY == 0) {
        SB();
#pragma unroll
        for (int ks = 0; ks < 2; ++ks)
#pragma unroll
          for (int ct = 0; ct < 4; ++ct) {
            LAS unsigned char* a = L + S_WX + (64 * (w >> 1) + 32 * ks + 8 * u + (n >> 2)) * 128 + ct * 32 + (n & 3) * 8;
            const v2u h0 = __builtin_bit_cast(v2u, __builtin_amdgcn_ds_read_tr16_b64_v4i16((LAS s4v*)a));
            const v2u h1 = __builtin_bit_cast(v2u, __builtin_amdgcn_ds_read_tr16_b64_v4i16((LAS s4v*)(a + 512)));
            wx[4 * ks + ct] = v4u{h0.x, h0.y, h1.x, h1.y};
          }
      }
      const float xres = (g == 0 && hh == 0) ? bflo(*(LAS u16*)(L + S_XB + (64 * cb + l) * 2)) : 0.f;
      bool bad = max(max(vmax(a0), vmax(a1)), max(ml.x, ml.y)) == SENT;
      if (LY == 0) REPOLL(34, bad, (a0 = ldx4(rws, o_cg, SC), a1 = ldx4(rws, o_cg + 1024, SC)), max(vmax(a0), vmax(a1)) == SENT);
      else REPOLL(42, bad, (ml = ldx2(rws, o_mg, SM), a0 = ldx4(rws, o_cg, SC), a1 = ldx4(rws, o_cg + 1024, SC)), max(max(vmax(a0), vmax(a1)), max(ml.x, ml.y)) == SENT);
      STAMP(5 + SO);
      f4 d[4];
#pragma unroll
      for (int ct = 0; ct < 4; ++ct) d[ct] = mfma(a0, wx[ct], f4{0.f, 0.f, 0.f, 0.f});
#pragma unroll
      for (int ct = 0; ct < 4; ++ct) d[ct] = mfma(a1, wx[4 + ct], d[ct]);
      LAS unsigned char* E = L + S_E + (LY == 0 ? 0 : w * 128);
      float e1 = 0.f;
      if (LY == 1) e1 = split_weights(ml, E, l);
      const f4 ev = *(LAS f4*)(E + (16 * (w & 1) + 4 * u) * 4);
      if (LY == 1 && LO) Ls = split_norm(e1, ml);
      float cs[4];
#pragma unroll
      for (int ct = 0; ct < 4; ++ct) cs[ct] = ev[0] * d[ct][0] + ev[1] * d[ct][1] + ev[2] * d[ct][2] + ev[3] * d[ct][3];
      const float z = sw16_add(sw32_add(cs[0], cs[2]), sw32_add(cs[1], cs[3]));
      *(LAS float*)(L + S_PART + w * 256 + l * 4) = z;
      BAR();
      if (LO && w == 0) {
        float s = 0.f;
#pragma unroll
        for (int i = 0; i < 8; ++i) s += *(LAS float*)(L + S_PART + i * 256 + l * 4);
        s = fmaf(s, __builtin_amdgcn_rcpf(Ls), xres);
        const u32 pk = cvtpk(s, dpp<QX1>(s));
#pragma unroll
        for (int r = 0; r < 8; ++r) { if (LY == 0) stx1(pk, rws, o_pub0, r * WD0_REP); else stx1(pk, rws, o_pub1, srep[r]); }
      }
      STAMP(6 + SO);
      if (LY == 1) return;
      if (LO) vote();
      if (LO && w == 1) {
        DLY(D_DSH);
        const u32 o = l < 16 ? W_D0 + g * WD0_REP + l * 896 + 6 * 128 : OOB;
        u32 v = ldx1(rws, o, 0);
        bool badv = v == SENT;
        REPOLL(39, badv, v = ldx1(rws, o, 0), v == SENT);
        if (l == 0) *(LAS u64*)(L + S_TML) = __builtin_amdgcn_s_memrealtime();
      }
      BAR();
      if (!LO) set_sides();
      wait_until(*(LAS u64*)(L + S_TML) + DLV(D_A1));
    } else {
      u64 t_ml = 0;
      if (LY == 0 && LO) vote();
      if (LO && w == 0) {
        v2u ml = ldx2(rws, o_mg, SM);
        bool bad = max(ml.x, ml.y) == SENT;
        REPOLL(34 + SO / 2, bad, ml = ldx2(rws, o_mg, SM), max(ml.x, ml.y) == SENT);
        t_ml = __builtin_amdgcn_s_memrealtime();
        if (l == 0) *(LAS u64*)(L + S_TML) = t_ml;
      }
      if (LY == 1) { BAR(); t_ml = *(LAS u64*)(L + S_TML); }
      STAMP(5 + SO);
      if (LY == 0) {
        if (LO) asm volatile("s_waitcnt vmcnt(0)" ::: "memory");
        if (ROLE == 2) {
          const rsrc_t rw2 = mkrs(p.w2, DM * FF * 2);
          const u32 o = (w < 7 && 128 * wa + 2 * l < DM) ? (224 * g + 32 * w) * (DM * 2) + wa * 256 + l * 4 : OOB;
#pragma unroll
          for (int i = 0; i < 32; ++i) w2_raw[i] = __builtin_amdgcn_raw_buffer_load_b32(rw2, o + i * (DM * 2), 0, NTW);
          SB();
          BAR();
        } else {
          if (!LO) {
            const int tt = t - 256;
            load_wx(WFE, 7);
            SB();
#pragma unroll
            for (int i = 0; i < 7; ++i) { const int idx = tt + 256 * i; *(LAS v4u*)(L + S_WX + ((idx ^ ((idx >> 5) & 7)) * 16)) = wx_raw[i]; }
          }
          BAR();
          v2u d[8];
#pragma unroll
          for (int jj = 0; jj < 8; ++jj) d[jj] = *(LAS v2u*)(L + S_WX + (((32 * lc + 4 * jj + (w >> 1)) ^ (lc & 7)) * 16) + (w & 1) * 8);
#pragma unroll
          for (int q = 0; q < 2; ++q)
#pragma unroll
            for (int i = 0; i < 4; ++i) { wx[2 * q][i] = __builtin_amdgcn_perm(d[2 * i + 1][q], d[2 * i][q], 0x05040100u); wx[2 * q + 1][i] = __builtin_amdgcn_perm(d[2 * i + 1][q], d[2 * i][q], 0x07060302u); }
          S13 = rows4(dot8(wx[0], onem4), dot8(wx[1], onem4), dot8(wx[2], onem4), dot8(wx[3], onem4));
        }
        t_ml = *(LAS u64*)(L + S_TML);
        if (!LO) set_sides();
      }
      wait_until(t_ml + DLV(LY == 0 ? D_DS0 : D_DS1));
      if (LY == 1) STAMP(49);
      v4u dv[4];
      auto dso = [&](int i) { return LY == 0 ? (u32)(i * 4 * 896) : sg + (i & 1) * 4096 + (i >> 1) * 16384; };
      if (LO) {
#pragma unroll
        for (int i = 0; i < 4; ++i) dv[i] = ldx4(rws, o_dg, dso(i));
      }
      if (LY == 0 && ROLE == 2) {
#pragma unroll
        for (int i = 0; i < 16; ++i) { wx[i >> 2][i & 3] = __builtin_amdgcn_perm(w2_raw[2 * i + 1], w2_raw[2 * i], 0x05040100u);
                                       wx[4 + (i >> 2)][i & 3] = __builtin_amdgcn_perm(w2_raw[2 * i + 1], w2_raw[2 * i], 0x07060302u); }
      }
      if (LO) {
        auto dbad = [&]() { return max(max(vmax(dv[0]), vmax(dv[1])), max(vmax(dv[2]), vmax(dv[3]))) == SENT; };
        bool bad = dbad();
        REPOLL(35 + SO / 2, bad, (dv[0] = ldx4(rws, o_dg, dso(0)), dv[1] = ldx4(rws, o_dg, dso(1)), dv[2] = ldx4(rws, o_dg, dso(2)), dv[3] = ldx4(rws, o_dg, dso(3))), dbad());
        STAMP(7 + SO);
        f4 x1 = {0.f, 0.f, 0.f, 0.f};
#pragma unroll
        for (int i = 0; i < 4; ++i) x1 = mfma(selm, dv[i], x1);
        if (a_xw < DM) {
          if (ROLE == 1) *(LAS v2u*)(L + S_XB + a_xw * 2) = v2u{cvtpk(x1[0], x1[1]), cvtpk(x1[2], x1[3])};
          else *(LAS f4*)(L + S_X1 + a_xw * 4) = x1;
        }
      }
      if constexpr (ROLE == 1) {
        BAR();
        const v4u xb = *(LAS v4u*)(L + S_XB + l * 16);
        float mu, r; ln_stats(xb, mu, r);
        float y = (rows4(dot8(xb, wx[0]), dot8(xb, wx[1]), dot8(xb, wx[2]), dot8(xb, wx[3])) - mu * S13) * r;
        if (!isup) y *= __builtin_amdgcn_rcpf(1.f + __builtin_amdgcn_exp2f(-1.4426950408889634f * y));
        auto s = __builtin_amdgcn_permlane16_swap(__float_as_uint(y), __float_as_uint(y), false, false);
        const u32 pk = cvtpk(__uint_as_float(s[0]), __uint_as_float(s[1]));
        auto s2 = __builtin_amdgcn_permlane32_swap(pk, pk, false, false);
        stx2(v2u{s2[0], s2[1]}, rws, o_pub0, SH + sg);
        STAMP(8 + SO);
        if (LY == 1) return;
        DLY(D_A2);
      } else {
        BAR();
        u32 sfin0 = 0, sfin1 = 0;
        if (LY == 1) {
#pragma unroll
          for (int r = 0; r < 4; ++r) if (r == wa) { sfin0 = srep[2 * r]; sfin1 = srep[2 * r + 1]; }
        }
        DLY(LY == 0 ? D_H0 : D_H1);
        u32 hg = ldx1(rws, o_gat, SH + sg), hu = ldx1(rws, o_gat, SH + sg + LH_UP);
        bool bad2 = max(hg, hu) == SENT;
        REPOLL(36 + SO / 2, bad2, (hg = ldx1(rws, o_gat, SH + sg), hu = ldx1(rws, o_gat, SH + sg + LH_UP)), max(hg, hu) == SENT);
        STAMP(8 + SO);
        const u32 hp = cvtpk(bflo(hg) * bflo(hu), bfhi(hg) * bfhi(hu));
        f2 acc = {0.f, 0.f};
        static_for<16>([&](auto I) { constexpr int i = decltype(I)::value;
          const u32 hb = (u32)__builtin_amdgcn_mov_dpp((int)hp, 0x150 + i, 0xf, 0xf, true);
          acc.x = dot2(hb, wx[i >> 2][i & 3], acc.x); acc.y = dot2(hb, wx[4 + (i >> 2)][i & 3], acc.y); });
        *(LAS f2*)(L + S_PART + w * 512 + l * 8) = acc;
        BAR();
        if (LO && w == 0) {
          f2 s = {0.f, 0.f};
          if (g == 0) s = *(LAS f2*)(L + S_X1 + min(128 * wa + 2 * l, DM - 2) * 4);
#pragma unroll
          for (int i = 0; i < 7; ++i) s += *(LAS f2*)(L + S_PART + i * 512 + l * 8);
          const u32 pk = cvtpk(s.x, s.y);
          if (LY == 0) {
#pragma unroll
            for (int r = 0; r < 8; ++r) stx1(pk, rws, o_pub0, srep[r]);
          } else {
            stx1(pk, rws, o_pub0, SA + sfin0); stx1(pk, rws, o_pub0, SA + sfin1);
          }
        }
        STAMP(9 + SO);
        if (LY == 1) {
          if (LO && w == 0 && j == 28 && g < 7) {
            DLY(D_F);
            const u32 o = n < 8 ? two(L_A + g * LA_REP + u * 1024 + g * 128 + n * 16) : OOB;
            v4u f0 = ldx4(rws, o, SA + sg), f1 = ldx4(rws, o, SA + sg + 4096);
            bool bad3 = max(vmax(f0), vmax(f1)) == SENT;
            REPOLL(45, bad3, (f0 = ldx4(rws, o, SA + sg), f1 = ldx4(rws, o, SA + sg + 4096)), max(vmax(f0), vmax(f1)) == SENT);
            const f4 y = mfma(selm, f1, mfma(selm, f0, f4{0.f, 0.f, 0.f, 0.f}));
            if (u < 2 && n < 8) *(v2u*)(p.out + 64 * g + 8 * n + 4 * u) = v2u{cvtpk(y[0], y[1]), cvtpk(y[2], y[3])};
            if (g == 0 && l == 0) stx1(T0, rws, WS_EP, 0);
            STAMP(10 + SO);
          }
          return;
        }
        DLY(D_A3);
      }
    }
    {
      if (LO) {
        v4u a0 = ldx4(rws, o_ag, sg), a1 = ldx4(rws, o_ag, sg + 4096);
        bool bad = max(vmax(a0), vmax(a1)) == SENT;
        REPOLL(37, bad, (a0 = ldx4(rws, o_ag, sg), a1 = ldx4(rws, o_ag, sg + 4096)), max(vmax(a0), vmax(a1)) == SENT);
        STAMP(10 + SO);
        const f4 x2 = mfma(selm, a1, mfma(selm, a0, f4{0.f, 0.f, 0.f, 0.f}));
        if (a_xw < DM) *(LAS v2u*)(L + S_XB + a_xw * 2) = v2u{cvtpk(x2[0], x2[1]), cvtpk(x2[2], x2[3])};
      }
      BAR();
      if (LO) {
        v4u xb = *(LAS v4u*)(L + S_XB + l * 16);
        const v2u qv = q_phase(xb);
        stx2(qv, rws, o_qs1, sg);
        STAMP(17);
        DLY(D_B1);
      }
    }
  };
  layer(std::integral_constant<int, 0>{});
  layer(std::integral_constant<int, 1>{});
  STAMP_FLUSH();
  asm volatile("; end of program %0 %1" :: "n"(ROLE), "n"(LO ? 1 : 0));
}

__global__ void __launch_bounds__(NT, 2) mega(Params p) {
  extern __shared__ __attribute__((aligned(16))) unsigned char lds_[];
  LAS unsigned char* L = (LAS unsigned char*)lds_;
  const int c = blockIdx.x, t = threadIdx.x, w = __builtin_amdgcn_readfirstlane(t >> 6), j = c >> 3;
  if (j < 14) { if (w < 4) program<0, true>(p, L, c, t, w); else program<0, false>(p, L, c, t, w); }
  else if (j < 28) { if (w < 4) program<1, true>(p, L, c, t, w); else program<1, false>(p, L, c, t, w); }
  else { if (w < 4) program<2, true>(p, L, c, t, w); else program<2, false>(p, L, c, t, w); }
}
}

extern "C" void kernel_launch(void* const* d_in, const int* in_sizes, int n_in, void* d_out, int out_size, void* d_ws, size_t ws_size, hipStream_t stream) {
  mk::Params p;
  p.x = (const mk::u16*)d_in[0]; p.k = (const mk::u16*)d_in[1]; p.v = (const mk::u16*)d_in[2];
  p.wq = (const mk::u16*)d_in[3]; p.wo = (const mk::u16*)d_in[4];
  p.w1 = (const mk::u16*)d_in[6]; p.w3 = (const mk::u16*)d_in[7]; p.w2 = (const mk::u16*)d_in[8];
  p.out = (mk::u16*)d_out; p.ws = (unsigned char*)d_ws;
  (void)hipFuncSetAttribute((const void*)mk::mega, hipFuncAttributeMaxDynamicSharedMemorySize, mk::LDS_BYTES);
  hipLaunchKernelGGL(mk::mega, dim3(256), dim3(mk::NT), mk::LDS_BYTES, stream, p);
}
```

```cpp
#include <hip/hip_runtime.h>
#include <stdint.h>
#include <type_traits>

namespace mk {
typedef unsigned u32; typedef unsigned short u16; typedef unsigned long long u64;
typedef u32 v4u __attribute__((ext_vector_type(4)));
typedef u32 v2u __attribute__((ext_vector_type(2)));
typedef float f2 __attribute__((ext_vector_type(2)));
typedef float f4 __attribute__((ext_vector_type(4)));
typedef short s4v __attribute__((ext_vector_type(4)));
typedef __bf16 bf2 __attribute__((ext_vector_type(2)));
typedef __bf16 bf8 __attribute__((ext_vector_type(8)));
#define LAS __attribute__((address_space(3)))
#define DEV __device__ __forceinline__

constexpr int DM = 448, SK = 8192, DH = 256, WIN = 2048, FF = 1792, NQ = 4096;
constexpr int NT = 512;
constexpr int WS_EP = 0;
constexpr int WS_RDY = 4096;
constexpr int W_Q0 = 8192, WQ0_GROUP = 8192;
constexpr int W_C0 = W_Q0 + 8 * WQ0_GROUP;
constexpr int W_ML0 = W_C0 + 262144;
constexpr int W_D0 = W_ML0 + 65536, WD0_REP = 16384;
constexpr int W_TWO = (W_D0 + 8 * WD0_REP + 16383) / 16384 * 16384;
__host__ __device__ constexpr int two(int o) { return W_TWO + ((o >> 13) << 14) + (o & 8191); }
constexpr int L_Q1 = 0, LQ1_GROUP = 4096;
constexpr int L_C1 = L_Q1 + 8 * LQ1_GROUP;
constexpr int L_ML1 = L_C1 + 262144;
constexpr int L_D1 = L_ML1 + 8192, LD1_REP = 16384;
constexpr int L_H = L_D1 + 8 * LD1_REP, LH_LAYER = 8192, LH_UP = 4096;
constexpr int L_A = L_H + 2 * LH_LAYER, LA_REP = 8192, LA_LAYER = 8 * LA_REP;
constexpr int L_PR = L_A + 2 * LA_LAYER;
constexpr int WS_BYTES = W_TWO + 2 * (L_PR + 131072);
static_assert(WS_BYTES <= (2 << 20), "one 2-MB page");
constexpr int WS_STAMP = 4 << 20;
constexpr u32 SENT = 0xFFFFFFFFu;
constexpr u32 OOB = 0x80000000u;

constexpr int S_WQ = 0;
constexpr int S_K = S_WQ + 16384;
constexpr int S_V = S_K + 32768;
constexpr int S_WX = S_V + 32768;
constexpr int S_Q = S_WX + 32768;
constexpr int S_SC = S_Q + 1280;
constexpr int S_P = S_SC + 640;
constexpr int S_E = S_P + 8 * 320;
constexpr int S_PART = S_E + 8 * 128;
constexpr int S_XB = S_PART + 4096;
constexpr int S_X1 = S_XB + 1024;
constexpr int S_HB = S_X1 + 1792;
constexpr int S_CNT = S_HB + 32;
constexpr int S_EP = S_HB + 48;
constexpr int S_TML = S_HB + 64;
constexpr int S_END = S_HB + 80;
constexpr int LDS_BYTES = (S_END + 255) / 256 * 256;
static_assert(LDS_BYTES > 81920 && LDS_BYTES <= 163840, "LDS must pin one workgroup per CU");

struct Params {
  const u16 *x, *k, *v, *wq, *wo, *w1, *w3, *w2;
  u16* out;
  unsigned char* ws;
};

#define SPIN_CAP (1u << 18)
#define BACKOFF 1
#define NTW 2
#define WARM 1
#define WARM_AUX 2
enum { D_B0, D_TB0, D_B1, D_C0, D_C1, D_DS0, D_DS1, D_H0, D_H1, D_A1, D_A2, D_A3, D_F, D_C0B, D_DSH, D_N };
#define DL_DEFAULT {20, 340, 18, 24, 18, 60, 62, 54, 48, 110, 60, 12, 8, 2, 30}
#define PROBE_THR 110
#define FUNC_END "mk_code_end"
#define CODEPF 1
#define WOE 8
#define WFE 4
#define DLV(k) dl_default[k]
constexpr int dl_default[D_N] = DL_DEFAULT;

DEV float bflo(u32 v) { return __uint_as_float(v << 16); }
DEV u32 cvtpk(float a, float b) { f2 v = {a, b}; bf2 r = __builtin_convertvector(v, bf2); return __builtin_bit_cast(u32, r); }
DEV float dot2(u32 a, u32 b, float c) { return __builtin_amdgcn_fdot2_f32_bf16(__builtin_bit_cast(bf2, a), __builtin_bit_cast(bf2, b), c, false); }
DEV f4 mfma(v4u a, v4u b, f4 c) { return __builtin_amdgcn_mfma_f32_16x16x32_bf16(__builtin_bit_cast(bf8, a), __builtin_bit_cast(bf8, b), c, 0, 0, 0); }
template <int C> DEV float dpp(float x) { return __builtin_bit_cast(float, __builtin_amdgcn_mov_dpp(__builtin_bit_cast(int, x), C, 0xf, 0xf, true)); }
constexpr int ROR8 = 0x128, ROR4 = 0x124, ROR2 = 0x122, ROR1 = 0x121, QX1 = 0xB1, QX2 = 0x4E, HMIR = 0x141;
DEV float sw32_add(float a, float b) { auto s = __builtin_amdgcn_permlane32_swap(__float_as_uint(a), __float_as_uint(b), false, false); return __uint_as_float(s[0]) + __uint_as_float(s[1]); }
DEV float sw16_add(float a, float b) { auto s = __builtin_amdgcn_permlane16_swap(__float_as_uint(a), __float_as_uint(b), false, false); return __uint_as_float(s[0]) + __uint_as_float(s[1]); }
DEV float vmaxf(float a, float b) { return __builtin_amdgcn_fmed3f(a, b, __builtin_inff()); }
DEV float sw32_max(float a, float b) { auto s = __builtin_amdgcn_permlane32_swap(__float_as_uint(a), __float_as_uint(b), false, false); return vmaxf(__uint_as_float(s[0]), __uint_as_float(s[1])); }
DEV float sw16_max(float a, float b) { auto s = __builtin_amdgcn_permlane16_swap(__float_as_uint(a), __float_as_uint(b), false, false); return vmaxf(__uint_as_float(s[0]), __uint_as_float(s[1])); }
DEV float row_sum(float z) { z += dpp<ROR8>(z); z += dpp<ROR4>(z); z += dpp<ROR2>(z); z += dpp<ROR1>(z); return z; }
DEV float row_max(float z) { z = vmaxf(z, dpp<ROR8>(z)); z = vmaxf(z, dpp<ROR4>(z)); z = vmaxf(z, dpp<ROR2>(z)); z = vmaxf(z, dpp<ROR1>(z)); return z; }
DEV float sum4(float a, float b, float c, float d) { return row_sum(sw16_add(sw32_add(a, c), sw32_add(b, d))); }
DEV float rdl(float v, int l) { return __uint_as_float(__builtin_amdgcn_readlane(__float_as_uint(v), l)); }
DEV void pin(u32& v) { asm volatile("" : "+v"(v)); }
DEV void pin(v4u& v) { asm volatile("" : "+v"(v)); }
#define SB() __builtin_amdgcn_sched_barrier(0)
#define BAR() do { asm volatile("s_waitcnt lgkmcnt(0)" ::: "memory"); __builtin_amdgcn_s_barrier(); asm volatile("" ::: "memory"); } while (0)
DEV void wait_until(u64 t) { asm volatile("" ::: "memory"); while (__builtin_amdgcn_s_memrealtime() < t) {} asm volatile("" ::: "memory"); SB(); }
#define DLY(k) wait_until(__builtin_amdgcn_s_memrealtime() + DLV(k))
#define CNT(i)
#define STAMP(i)
#define STAMP_FLUSH()
#define REPOLL(id, bad, LOADS, BAD) do { if (__builtin_expect(__any(bad), 0)) { for (u32 sp_ = 0; sp_ < SPIN_CAP; ++sp_) { asm volatile("" ::: "memory"); CNT(id); \
    if (BACKOFF) __builtin_amdgcn_s_sleep(BACKOFF); LOADS; if (!__any(BAD)) break; } } asm volatile("" ::: "memory"); } while (0)

typedef __amdgpu_buffer_rsrc_t rsrc_t;
DEV rsrc_t mkrs(const void* p, u32 bytes) { return __builtin_amdgcn_make_buffer_rsrc((void*)p, 0, bytes, 0x00020000); }
DEV v4u ldw(rsrc_t r, u32 off) { return __builtin_amdgcn_raw_buffer_load_b128(r, off, 0, NTW); }
DEV v4u ldx4(rsrc_t r, u32 off, u32 so) { return __builtin_amdgcn_raw_buffer_load_b128(r, off, so, 16); }
DEV v2u ldx2(rsrc_t r, u32 off, u32 so) { return __builtin_amdgcn_raw_buffer_load_b64(r, off, so, 16); }
DEV u32 ldx1(rsrc_t r, u32 off, u32 so) { return __builtin_amdgcn_raw_buffer_load_b32(r, off, so, 16); }
DEV void stx4(v4u v, rsrc_t r, u32 off, u32 so) { __builtin_amdgcn_raw_buffer_store_b128(v, r, off, so, 16); }
DEV void stx2(v2u v, rsrc_t r, u32 off, u32 so) { __builtin_amdgcn_raw_buffer_store_b64(v, r, off, so, 16); }
DEV void stx1(u32 v, rsrc_t r, u32 off, u32 so) { __builtin_amdgcn_raw_buffer_store_b32(v, r, off, so, 16); }
DEV u32 vmax(v4u v) { return max(max(max(v.x, v.y), v.z), v.w); }
constexpr u32 ONE = 0x3F803F80u;

DEV void read_cols(LAS unsigned char* base, int lc, int w, v4u& wa, v4u& wb) {
  u32 d[8];
#pragma unroll
  for (int jj = 0; jj < 8; ++jj) d[jj] = *(LAS u32*)(base + (((16 * lc + 2 * jj + (w >> 2)) ^ (lc & 7)) * 16) + (w & 3) * 4);
#pragma unroll
  for (int i = 0; i < 4; ++i) { wa[i] = __builtin_amdgcn_perm(d[2 * i + 1], d[2 * i], 0x05040100u); wb[i] = __builtin_amdgcn_perm(d[2 * i + 1], d[2 * i], 0x07060302u); }
}
DEV float dot8(const v4u x, const v4u wv) { float a = 0.f;
#pragma unroll
  for (int i = 0; i < 4; ++i) a = dot2(x[i], wv[i], a);
  return a; }
DEV float rows4(float a, float b, float c, float d) { return row_sum(sw16_add(sw32_add(a, c), sw32_add(b, d))); }
DEV void ln_stats(const v4u x, float& mu, float& r) {
  float st = sw32_add(dot8(x, v4u{ONE, ONE, ONE, ONE}), dot8(x, x)); st = row_sum(sw16_add(st, st));
  mu = rdl(st, 0) * (1.f / DM);
  r = __builtin_amdgcn_rsqf(fmaxf(fmaf(rdl(st, 32), 1.f / DM, -mu * mu), 0.f) + 1e-5f);
}
DEV float reduce_rows(const float (&v8)[8], int l) {
  float v4_[4], v2_[2];
#pragma unroll
  for (int i = 0; i < 4; ++i) v4_[i] = sw32_add(v8[i], v8[i + 4]);
#pragma unroll
  for (int i = 0; i < 2; ++i) v2_[i] = sw16_add(v4_[i], v4_[i + 2]);
  const bool k8 = l & 8;
  return (k8 ? v2_[1] : v2_[0]) + dpp<ROR8>(k8 ? v2_[0] : v2_[1]);
}
template <int N, int I = 0, class F> DEV void static_for(F&& f) { if constexpr (I < N) { f(std::integral_constant<int, I>{}); static_for<N, I + 1>(f); } }
DEV float split_weights(const v2u ml, LAS unsigned char* E, int l) {
  const float mv = __uint_as_float(ml.x);
  float M = row_max(mv); M = sw16_max(M, M);
  const float e = __builtin_amdgcn_exp2f(mv - M);
  *(LAS float*)(E + (l & 31) * 4) = e;
  return e;
}
DEV float split_norm(float e, const v2u ml) { float Ls = row_sum(e * __uint_as_float(ml.y)); return sw16_add(Ls, Ls); }
DEV v4u sel_matrix(int n) { v4u a = {0u, 0u, 0u, 0u};
#pragma unroll
  for (int i = 0; i < 4; ++i) a[i] = (n >> 1) == i ? ((n & 1) ? 0x3F800000u : 0x00003F80u) : 0u;
  return a; }
DEV float bfhi(u32 v) { return __uint_as_float(v & 0xFFFF0000u); }

template <int ROLE, bool LO>
DEV void program(const Params& p, LAS unsigned char* L, const int c, const int t, const int w, const u64 code, const u32 code_len) {
  const int g = c & 7, j = c >> 3, l = t & 63, u = l >> 4, n = l & 15;
  asm volatile("; start of program %0 %1" :: "n"(ROLE), "n"(LO ? 1 : 0) : "memory");
  STAMP(0);
  const u64 t_start = LO ? __builtin_amdgcn_s_memrealtime() : 0;
  const rsrc_t rws = mkrs(p.ws, WS_BYTES);
  const int hh = j >= 7, h = 2 * g + hh, cb = j - 7 * hh;
  const bool isup = j >= 21;
  const int ub = 7 * g + (j - (isup ? 21 : 14));
  const int wa = j - 28;
  const int kq = j >> 3, cq = j & 7;
  const u32 kvbase = ((u32)g * SK + (SK - WIN) + j * 64) * (DH * 2);

  v4u xp = {0, 0, 0, 0}, ep = {0, 0, 0, 0}, wq_raw[4];
  u32 code_pf = 0;
  if constexpr (LO) {
    const rsrc_t rwq = mkrs(p.wq, DM * NQ * 2);
    const u32 o = (112 * kq + (t >> 3)) * (NQ * 2) + (g * 512 + cq * 64) * 2 + (t & 7) * 16;
#pragma unroll
    for (int i = 0; i < 4; ++i) wq_raw[i] = ldw(rwq, (i < 3 || t < 128) ? o + i * 32 * (NQ * 2) : OOB);
    xp = __builtin_amdgcn_raw_buffer_load_b128(mkrs(p.x, DM * 2), w == 0 ? l * 16 : OOB, 0, 0);
    ep = ldx4(rws, (w == 0 && l == 0) ? WS_EP : OOB, 0);
    SB();
    u32 oc = OOB;
    if (w == 0) oc = W_C0 + g * 32768 + j * 1024 + l * 16;
    if (w == 1 && ROLE == 0) oc = W_D0 + (l >> 3) * WD0_REP + h * 896 + cb * 128 + (l & 7) * 16;
    if (w == 2 && l < 8) oc = two(L_PR + c * 512 + (l >> 1) * 128) + (l & 1) * 8192;
    if (w == 3 && l < 2) oc = W_ML0 + ((2 * g + l) * 32 + j) * 128;
    stx4(v4u{SENT, SENT, SENT, SENT}, rws, oc, 0);
    if (ROLE != 0) {
      oc = OOB;
      if (ROLE == 1 && w == 0 && l < 8) oc = two(L_H + (isup ? LH_UP : 0) + ub * 64 + (l & 3) * 16) + (l >> 2) * 8192;
      if (ROLE == 2 && (l & 15) + 16 * wa < 56) oc = two(L_A + (2 * w + (l >> 5)) * LA_REP + g * 1024 + wa * 256 + (l & 15) * 16) + ((l >> 4) & 1) * 8192;
      stx4(v4u{SENT, SENT, SENT, SENT}, rws, oc, 0);
    }
    if (t == 0) *(LAS v4u*)(L + S_CNT) = v4u{0u, 0u, 0u, 0u};
    asm volatile("s_waitcnt lgkmcnt(0)" ::: "memory");
  }
  if constexpr (!LO) {
    if (CODEPF) code_pf = __builtin_amdgcn_raw_buffer_load_b32(mkrs((const void*)code, code_len & ~3u), w == 7 ? (j + 32 * l) * 128 : OOB, 0, 0);
  }
  SB();
  __builtin_amdgcn_s_barrier();
  SB();
  STAMP(12);
  v4u k_raw[8], v_raw[8], wx_raw[8];
  if constexpr (LO) {
    const rsrc_t rk = mkrs(p.k, 8u * SK * DH * 2);
#pragma unroll
    for (int i = 0; i < 8; ++i) k_raw[i] = ldw(rk, kvbase + (t + 256 * i) * 16);
  } else {
    const int tt = t - 256;
    const rsrc_t rv = mkrs(p.v, 8u * SK * DH * 2);
#pragma unroll
    for (int i = 0; i < 8; ++i) v_raw[i] = ldw(rv, kvbase + (tt + 256 * i) * 16);
    STAMP(29);
  }
  SB();
  STAMP(13);
  u32 o_qs0 = n == 0 ? W_Q0 + g * WQ0_GROUP + kq * 2048 + (16 * cq + 4 * w + u) * 16 : OOB;
  u32 o_qs1 = n == 0 ? two(L_Q1 + g * LQ1_GROUP + kq * 1024 + (64 * cq + 16 * w + 4 * u) * 2) : OOB;
  u32 o_qg0 = W_Q0 + g * WQ0_GROUP + (l >> 5) * 4096 + (32 * w + (l & 31)) * 16;
  u32 o_qg1 = two(L_Q1 + g * LQ1_GROUP + u * 1024 + (128 * w + 8 * n) * 2);
  const int cofs = (2 * g + n) * 16384 + (j >> 4) * 8192 + w * 1024 + (u * 16 + (j & 15)) * 16;
  u32 o_cs0 = n < 2 ? W_C0 + cofs : OOB, o_cs1 = n < 2 ? two(L_C1 + cofs) : OOB;
  u32 o_ms0 = (l & 31) == 0 ? W_ML0 + ((2 * g + (l >> 5)) * 32 + j) * 128 : OOB, o_ms1 = (l & 31) == 0 ? two(L_ML1 + ((2 * g + (l >> 5)) * 32 + j) * 8) : OOB;
  const int hx = ROLE == 0 ? h : 2 * g;
  const int gofs = hx * 16384 + (w & 1) * 8192 + (w >> 1) * 2048 + l * 16;
  u32 o_cg0 = W_C0 + gofs, o_cg1 = two(L_C1 + gofs);
  u32 o_mg0 = W_ML0 + (hx * 32 + (l & 31)) * 128, o_mg1 = two(L_ML1 + (hx * 32 + (l & 31)) * 8);
  u32 o_pub0 = OOB, o_pub1 = OOB, o_gat = OOB;
  if (ROLE == 0) { o_pub0 = (l & 1) == 0 ? W_D0 + h * 896 + cb * 128 + (l >> 1) * 4 : OOB;
                   o_pub1 = (l & 1) == 0 ? two(L_D1 + h * 1024 + cb * 128 + (l >> 1) * 4) : OOB; }
  else if (ROLE == 1) o_pub0 = l == 0 ? two(L_H + (isup ? LH_UP : 0) + ub * 64 + w * 8) : OOB;
  else { o_pub0 = 128 * wa + 2 * l < DM ? two(L_A + g * 1024 + wa * 256 + l * 4) : OOB;
         o_gat = w < 7 ? two(L_H + (224 * g + 32 * w) * 2 + n * 4) : OOB; }
  const bool cin = LO && 128 * w + 8 * n < DM;
  u32 o_dg0 = cin ? W_D0 + g * WD0_REP + u * 896 + w * 256 + n * 16 : OOB;
  u32 o_dg1 = cin ? two(L_D1 + g * LD1_REP + u * 1024 + w * 256 + n * 16) : OOB;
  u32 o_ag = cin ? two(L_A + g * LA_REP + u * 1024 + w * 256 + n * 16) : OOB;
  u32 a_xw = (cin && u < 2) ? (128 * w + 8 * n + 4 * u) : 0xFFFF;
  v4u selm = sel_matrix(n);
  pin(o_cs0); pin(o_cs1); pin(o_mg0); pin(o_mg1); pin(o_pub0);
  if (LO) { pin(o_ag); pin(a_xw); pin(selm); pin(o_qs0); pin(o_qs1); pin(o_qg0); pin(o_qg1); pin(o_ms0); pin(o_ms1); }
  if (ROLE == 0) { pin(o_cg0); pin(o_cg1); pin(o_pub1); }
  if (ROLE == 2) pin(o_gat);
  if (ROLE != 0 && LO) { pin(o_dg0); pin(o_dg1); }
  const int lc = min(l, 55);
  const u32 onem = l < 56 ? ONE : 0u;
  const v4u onem4 = {onem, onem, onem, onem};
  SB();
  u32 T0 = 0, T1 = 0, R0 = 0, R1 = 0, R2 = 0, R3 = 0;
  auto lo_sync = [&](u32 target) {
    asm volatile("s_waitcnt lgkmcnt(0)" ::: "memory");
    if (l == 0) __hip_atomic_fetch_add((LAS u32*)(L + S_CNT), 1u, __ATOMIC_RELAXED, __HIP_MEMORY_SCOPE_WORKGROUP);
    while (*(volatile LAS u32*)(L + S_CNT) < target) {}
    asm volatile("" ::: "memory");
  };
  v4u kf[8];
  v4u wqf[4];
  f4 Sq;
  auto q_phase = [&](const v4u xs) {
    f4 acc = {0.f, 0.f, 0.f, 0.f};
#pragma unroll
    for (int s = 0; s < 4; ++s) acc = mfma(wqf[s], *(LAS v4u*)(L + S_XB + (112 * kq + 32 * s + 8 * u) * 2), acc);
    float mu, r; ln_stats(xs, mu, r);
    return v2u{cvtpk((acc[0] - mu * Sq[0]) * r, (acc[1] - mu * Sq[1]) * r), cvtpk((acc[2] - mu * Sq[2]) * r, (acc[3] - mu * Sq[3]) * r)};
  };
  if constexpr (LO) {
#pragma unroll
    for (int i = 0; i < 4; ++i) *(LAS v4u*)(L + S_WQ + (t + 256 * i) * 16) = wq_raw[i];
    if (w == 0) { *(LAS v4u*)(L + S_XB + l * 16) = xp; if (l == 0) *(LAS v4u*)(L + S_EP) = ep; }
    lo_sync(4);
    STAMP(11);
    xp = *(LAS v4u*)(L + S_XB + l * 16);
    { const v4u e = *(LAS v4u*)(L + S_EP);
      T0 = __builtin_amdgcn_readfirstlane(e.x) + 1u; T1 = __builtin_amdgcn_readfirstlane(e.y); const u32 T2 = __builtin_amdgcn_readfirstlane(e.z);
      R0 = T0 << 3; R1 = (T0 * 0x9E3779B1u ^ T1) & ~7u; R2 = (T0 * 0x85EBCA77u ^ T2) & ~7u; R3 = (T0 * 0xC2B2AE3Du ^ T1 ^ (T2 >> 7)) & ~7u; }
#pragma unroll
    for (int s = 0; s < 4; ++s) {
      LAS unsigned char* a = L + S_WQ + (32 * s + 8 * u + (n >> 2)) * 128 + w * 32 + (n & 3) * 8;
      const v2u h0 = __builtin_bit_cast(v2u, __builtin_amdgcn_ds_read_tr16_b64_v4i16((LAS s4v*)a));
      const v2u h1 = __builtin_bit_cast(v2u, __builtin_amdgcn_ds_read_tr16_b64_v4i16((LAS s4v*)(a + 512)));
      wqf[s] = v4u{h0.x, h0.y, h1.x, h1.y};
    }
    Sq = f4{0.f, 0.f, 0.f, 0.f};
#pragma unroll
    for (int s = 0; s < 4; ++s) Sq = mfma(wqf[s], v4u{ONE, ONE, ONE, ONE}, Sq);
    { const v2u qv = q_phase(xp);
      asm volatile("s_waitcnt vmcnt(8)" ::: "memory");
      stx4(v4u{qv.x, qv.y, T0, T1}, rws, o_qs0, 0); }
    STAMP(1);
#pragma unroll
    for (int i = 0; i < 8; ++i) { const int idx = t + 256 * i, key = idx >> 5; *(LAS v4u*)(L + S_K + key * 512 + (((idx & 31) ^ (key & 15)) * 16)) = k_raw[i]; }
    lo_sync(8);
#pragma unroll
    for (int s = 0; s < 8; ++s) kf[s] = *(LAS v4u*)(L + S_K + (16 * w + n) * 512 + (((4 * s + u) ^ n) * 16));
    {
      const u32 first = (j ^ w) & 1, oa = two(L_PR + c * 512 + w * 128) + first * 8192, ob = two(L_PR + c * 512 + w * 128) + (first ^ 1) * 8192;
      u32 va = ldx1(rws, l == 0 ? oa : OOB, 0), vb = ldx1(rws, l == 0 ? ob : OOB, 0);
      SB();
      asm volatile("s_waitcnt vmcnt(1)" : "+v"(va) :: "memory");
      const u64 t1 = __builtin_amdgcn_s_memtime();
      asm volatile("s_waitcnt lgkmcnt(0)\n\ts_waitcnt vmcnt(0)" : "+v"(vb) :: "memory");
      const u64 t2 = __builtin_amdgcn_s_memtime();

      u32 xcc; asm volatile("s_getreg_b32 %0, hwreg(HW_REG_XCC_ID, 0, 4)" : "=s"(xcc));
      const u32 die = (xcc >> 2) & 1;
      const u32 vt = (u32)(t2 - t1) > PROBE_THR ? ((first ^ die) ? 1u : 2u) : 0u;
      __builtin_amdgcn_raw_buffer_store_b32((w == 0 ? R0 : w == 1 ? R1 : w == 2 ? R2 : R3) | (die << 2) | vt, rws, l == 0 ? WS_RDY + c * 16 + w * 4 : OOB, 0, 16);
    }
    STAMP(14);
  }

  u32 sg = 0, srep[8];
  auto set_sides = [&]() {
    const v4u cw = *(LAS v4u*)(L + S_CNT);
    const u32 sm = __builtin_amdgcn_readfirstlane(((int)cw.z > 0 ? ~cw.w : cw.w) & 0xFFu);
    sg = ((sm >> g) & 1u) << 13;
#pragma unroll
    for (int r = 0; r < 8; ++r) srep[r] = r * (ROLE == 0 ? 2 * LD1_REP : 2 * LA_REP) + (((sm >> r) & 1u) << 13);
  };
  auto vote = [&]() {
    v4u rdy = ldx4(rws, WS_RDY + t * 16, 0);
    auto rbad = [&]() { return ((rdy.x & ~7u) != R0) | ((rdy.y & ~7u) != R1) | ((rdy.z & ~7u) != R2) | ((rdy.w & ~7u) != R3); };
    bool bad = rbad();
    REPOLL(33, bad, rdy = ldx4(rws, WS_RDY + t * 16, 0), rbad());
    const int votes = __popcll(__ballot(rdy.x & 1u)) + __popcll(__ballot(rdy.y & 1u)) + __popcll(__ballot(rdy.z & 1u)) + __popcll(__ballot(rdy.w & 1u))
                    - __popcll(__ballot(rdy.x & 2u)) - __popcll(__ballot(rdy.y & 2u)) - __popcll(__ballot(rdy.z & 2u)) - __popcll(__ballot(rdy.w & 2u));
    const u64 dies = __ballot(rdy.w & 4u);
    if (l == 0) { __hip_atomic_fetch_add((LAS u32*)(L + S_CNT + 8), (u32)votes, __ATOMIC_RELAXED, __HIP_MEMORY_SCOPE_WORKGROUP);
                  if (w == 0) *(LAS u32*)(L + S_CNT + 12) = (u32)dies & 0xFFu; }
    lo_sync(16);
    set_sides();
    if (ROLE != 0) {
      const int m = j - 14, ch = 2 * m + w;
      u32 oc = OOB;
      if (w < 2) oc = two(ch < 32 ? L_C1 + g * 32768 + ch * 1024 + l * 16 : L_Q1 + g * LQ1_GROUP + (ch - 32) * 1024 + l * 16);
      else if (w == 2) {
        if (m < 16) oc = two(L_D1 + g * LD1_REP + m * 1024 + l * 16);
        else if (m == 16) { if (l < 32) oc = two(L_ML1 + g * 512 + l * 16); }
        else if ((l & 31) < 28) oc = two(L_H + LH_LAYER + (l >> 5) * LH_UP + g * 448 + (l & 31) * 16);
      } else if (m < 8) oc = two(L_A + LA_LAYER + g * LA_REP + m * 1024 + l * 16);
      stx4(v4u{SENT, SENT, SENT, SENT}, rws, oc == OOB ? OOB : oc + sg, 0);
    }
  };

  v4u vf[2][2];
  float S13 = 0.f;
  v4u wx[8];
  u32 w2_raw[32];

  auto load_wx = [&](int i0, int i1) {
    const int tt = t - 256;
    if (ROLE == 0) {
      const rsrc_t rwo = mkrs(p.wo, DM * NQ * 2);
#pragma unroll
      for (int i = 0; i < 8; ++i) if (i >= i0 && i < i1) wx_raw[i] = ldw(rwo, (h * 256 + (tt >> 3) + 32 * i) * (DM * 2) + cb * 128 + (tt & 7) * 16);
    } else if (ROLE == 1) {
      const rsrc_t r1 = mkrs(isup ? p.w3 : p.w1, DM * FF * 2);
      const u32 o = (tt >> 2) * (FF * 2) + ub * 64 + (tt & 3) * 16;
#pragma unroll
      for (int i = 0; i < 7; ++i) if (i >= i0 && i < i1) wx_raw[i] = ldw(r1, o + i * 64 * (FF * 2));
    }
  };

  auto layer = [&](auto LC) {
    constexpr int LY = decltype(LC)::value;
    constexpr u32 SH = LY * 2 * LH_LAYER, SA = LY * 2 * LA_LAYER;
    constexpr int SO = 16 * LY;
    const u32 o_ms = LY == 0 ? o_ms0 : o_ms1, o_mg = LY == 0 ? o_mg0 : o_mg1, o_cs = LY == 0 ? o_cs0 : o_cs1, o_cg = LY == 0 ? o_cg0 : o_cg1, o_dg = LY == 0 ? o_dg0 : o_dg1;
    const u32 SM = LY == 0 ? 0u : sg, SC = SM;
    if (LO) {
      if (LY == 0) {
        DLY(D_B0);
        wait_until(t_start + DLV(D_TB0));
        auto qbad = [&](const v4u& q) { return (q.z != T0) | (q.w != T1); };
        v4u q0 = ldx4(rws, o_qg0, 0), q1 = ldx4(rws, o_qg0 + 2048, 0);
        SB();
        if (l == 0) __hip_atomic_fetch_add((LAS u32*)(L + S_CNT + 4), 1u, __ATOMIC_RELAXED, __HIP_MEMORY_SCOPE_WORKGROUP);
        SB();
        bool bad = qbad(q0) | qbad(q1);
        REPOLL(32, bad, (q0 = ldx4(rws, o_qg0, 0), q1 = ldx4(rws, o_qg0 + 2048, 0)), qbad(q0) | qbad(q1));
        const float c01 = sw32_add(bflo(q0.x) + bflo(q1.x), bflo(q0.y) + bflo(q1.y));
        const float c13 = sw32_add(bfhi(q0.x) + bfhi(q1.x), bfhi(q0.y) + bfhi(q1.y));
        *(LAS u32*)(L + S_Q + (w >> 1) * 576 + ((32 * w + (l & 31)) & 63) * 8 + (l >> 5) * 4) = cvtpk(c01, c13);
        lo_sync(12);
      } else {
        if (LY == 1) STAMP(50);
        v4u q0 = ldx4(rws, o_qg1, sg);
        bool bad = vmax(q0) == SENT;
        REPOLL(40, bad, q0 = ldx4(rws, o_qg1, sg), vmax(q0) == SENT);
        const f4 s = mfma(selm, q0, f4{0.f, 0.f, 0.f, 0.f});
        if (u < 2) *(LAS v2u*)(L + S_Q + (w >> 1) * 576 + ((128 * w + 8 * n + 4 * u) & 255) * 2) = v2u{cvtpk(s[0], s[1]), cvtpk(s[2], s[3])};
      }
    } else if (LY == 0) {
      if ((ROLE == 0 && WOE > 0) || (ROLE == 1 && WFE > 0)) {
        while (*(volatile LAS u32*)(L + S_CNT + 4) < 4u) __builtin_amdgcn_s_sleep(1);
        asm volatile("" ::: "memory");
        load_wx(0, ROLE == 0 ? WOE : WFE);
      }
      const int tt = t - 256;
      SB();
      STAMP(30);
#pragma unroll
      for (int i = 0; i < 8; ++i) { const int idx = tt + 256 * i, row = idx >> 5, gr = idx & 31; *(LAS v4u*)(L + S_V + row * 512 + ((gr ^ (4 * (row & 3))) * 16)) = v_raw[i]; }
      STAMP(31);
      if (CODEPF) { u32 tmp_ = code_pf; pin(tmp_); }
    }
    if (LY == 1) BAR();
    STAMP(2 + SO);
    if (LO) {
      LAS unsigned char* Q = L + S_Q;
      f4 acc = {0.f, 0.f, 0.f, 0.f};
#pragma unroll
      for (int s = 0; s < 8; ++s) { const v4u qf = *(LAS v4u*)(Q + (n & 1) * 576 + s * 64 + u * 16); acc = mfma(kf[s], qf, acc); }
      if (n < 2) *(LAS f4*)(L + S_SC + n * 272 + (16 * w + 4 * u) * 4) = acc;
    }
    BAR();
    STAMP(3 + SO);
    if (LY == 0) {
#pragma unroll
      for (int tl = 0; tl < 2; ++tl)
#pragma unroll
        for (int st = 0; st < 2; ++st) {
          const int key = 32 * st + 8 * u + (n >> 2), gr = 4 * w + (n & 3);
          LAS unsigned char* a = L + S_V + key * 512 + ((gr ^ (4 * (key & 3))) * 16) + tl * 8;
          const v2u h0 = __builtin_bit_cast(v2u, __builtin_amdgcn_ds_read_tr16_b64_v4i16((LAS s4v*)a));
          const v2u h1 = __builtin_bit_cast(v2u, __builtin_amdgcn_ds_read_tr16_b64_v4i16((LAS s4v*)(a + 2048)));
          vf[tl][st] = v4u{h0.x, h0.y, h1.x, h1.y};
        }
    }
    {
      const float s0 = *(LAS float*)(L + S_SC + l * 4), s1 = *(LAS float*)(L + S_SC + 272 + l * 4);
      constexpr float CE = 0.0625f * 1.4426950408889634f;
      float m = row_max(sw32_max(s0, s1));
      m = sw16_max(m, m) * CE;
      auto ms = __builtin_amdgcn_permlane32_swap(__float_as_uint(m), __float_as_uint(m), false, false);
      const float p0 = __builtin_amdgcn_exp2f(fmaf(s0, CE, -__uint_as_float(ms[0]))), p1 = __builtin_amdgcn_exp2f(fmaf(s1, CE, -__uint_as_float(ms[1])));
      const u32 pk = cvtpk(p0, p1);
      *(LAS u16*)(L + S_P + w * 320 + l * 2) = (u16)pk;
      *(LAS u16*)(L + S_P + w * 320 + 160 + l * 2) = (u16)(pk >> 16);
      const v4u pf0 = *(LAS v4u*)(L + S_P + w * 320 + (n & 1) * 160 + u * 16), pf1 = *(LAS v4u*)(L + S_P + w * 320 + (n & 1) * 160 + 64 + u * 16);
      f4 o0 = {0.f, 0.f, 0.f, 0.f}, o1 = o0;
      o0 = mfma(vf[0][0], pf0, o0); o1 = mfma(vf[1][0], pf0, o1);
      o0 = mfma(vf[0][1], pf1, o0); o1 = mfma(vf[1][1], pf1, o1);
      stx4(v4u{cvtpk(o0[0], o0[1]), cvtpk(o0[2], o0[3]), cvtpk(o1[0], o1[1]), cvtpk(o1[2], o1[3])}, rws, o_cs, SC);
      if (LO) {
        float ls = row_sum(sw32_add(p0, p1)); ls = sw16_add(ls, ls);
        stx2(v2u{__float_as_uint(m), __float_as_uint(ls)}, rws, w == 0 ? o_ms : OOB, SM);
      }
    }
    STAMP(4 + SO);
    if (!(LY == 0 && !LO && ROLE != 0)) DLY(LY == 0 ? D_C0 : D_C1);
    if constexpr (ROLE == 0) {
      float Ls = 1.f;
      if (LY == 0) {
        if (LO) {
          if (w == 0) {
            v2u m0 = ldx2(rws, o_mg, SM);
            bool badm = max(m0.x, m0.y) == SENT;
            REPOLL(38, badm, m0 = ldx2(rws, o_mg, SM), max(m0.x, m0.y) == SENT);
            Ls = split_norm(split_weights(m0, L + S_E, l), m0);
          }
          STAMP(53);
        } else {
          load_wx(WOE, 8);
          SB();
#pragma unroll
          for (int i = 0; i < 8; ++i) *(LAS v4u*)(L + S_WX + (t - 256 + 256 * i) * 16) = wx_raw[i];
        }
        BAR();
        STAMP(54);
        DLY(D_C0B);
      }
      if (LY == 1) STAMP(48);
      v2u ml = {0u, 0u};
      if (LY == 1) ml = ldx2(rws, o_mg, SM);
      v4u a0 = ldx4(rws, o_cg, SC), a1 = ldx4(rws, o_cg + 1024, SC);
      if (LY == 0) {
        SB();
#pragma unroll
        for (int ks = 0; ks < 2; ++ks)
#pragma unroll
          for (int ct = 0; ct < 4; ++ct) {
            LAS unsigned char* a = L + S_WX + (64 * (w >> 1) + 32 * ks + 8 * u + (n >> 2)) * 128 + ct * 32 + (n & 3) * 8;
            const v2u h0 = __builtin_bit_cast(v2u, __builtin_amdgcn_ds_read_tr16_b64_v4i16((LAS s4v*)a));
            const v2u h1 = __builtin_bit_cast(v2u, __builtin_amdgcn_ds_read_tr16_b64_v4i16((LAS s4v*)(a + 512)));
            wx[4 * ks + ct] = v4u{h0.x, h0.y, h1.x, h1.y};
          }
      }
      const float xres = (g == 0 && hh == 0) ? bflo(*(LAS u16*)(L + S_XB + (64 * cb + l) * 2)) : 0.f;
      bool bad = max(max(vmax(a0), vmax(a1)), max(ml.x, ml.y)) == SENT;
      if (LY == 0) REPOLL(34, bad, (a0 = ldx4(rws, o_cg, SC), a1 = ldx4(rws, o_cg + 1024, SC)), max(vmax(a0), vmax(a1)) == SENT);
      else REPOLL(42, bad, (ml = ldx2(rws, o_mg, SM), a0 = ldx4(rws, o_cg, SC), a1 = ldx4(rws, o_cg + 1024, SC)), max(max(vmax(a0), vmax(a1)), max(ml.x, ml.y)) == SENT);
      STAMP(5 + SO);
      f4 d[4];
#pragma unroll
      for (int ct = 0; ct < 4; ++ct) d[ct] = mfma(a0, wx[ct], f4{0.f, 0.f, 0.f, 0.f});
#pragma unroll
      for (int ct = 0; ct < 4; ++ct) d[ct] = mfma(a1, wx[4 + ct], d[ct]);
      LAS unsigned char* E = L + S_E + (LY == 0 ? 0 : w * 128);
      float e1 = 0.f;
      if (LY == 1) e1 = split_weights(ml, E, l);
      const f4 ev = *(LAS f4*)(E + (16 * (w & 1) + 4 * u) * 4);
      if (LY == 1 && LO) Ls = split_norm(e1, ml);
      float cs[4];
#pragma unroll
      for (int ct = 0; ct < 4; ++ct) cs[ct] = ev[0] * d[ct][0] + ev[1] * d[ct][1] + ev[2] * d[ct][2] + ev[3] * d[ct][3];
      const float z = sw16_add(sw32_add(cs[0], cs[2]), sw32_add(cs[1], cs[3]));
      *(LAS float*)(L + S_PART + w * 256 + l * 4) = z;
      BAR();
      if (LO && w == 0) {
        float s = 0.f;
#pragma unroll
        for (int i = 0; i < 8; ++i) s += *(LAS float*)(L + S_PART + i * 256 + l * 4);
        s = fmaf(s, __builtin_amdgcn_rcpf(Ls), xres);
        const u32 pk = cvtpk(s, dpp<QX1>(s));
#pragma unroll
        for (int r = 0; r < 8; ++r) { if (LY == 0) stx1(pk, rws, o_pub0, r * WD0_REP); else stx1(pk, rws, o_pub1, srep[r]); }
      }
      STAMP(6 + SO);
      if (LY == 1) return;
      if (LO) vote();
      if (LO && w == 1) {
        DLY(D_DSH);
        const u32 o = l < 16 ? W_D0 + g * WD0_REP + l * 896 + 6 * 128 : OOB;
        u32 v = ldx1(rws, o, 0);
        bool badv = v == SENT;
        REPOLL(39, badv, v = ldx1(rws, o, 0), v == SENT);
        if (l == 0) *(LAS u64*)(L + S_TML) = __builtin_amdgcn_s_memrealtime();
      }
      BAR();
      if (!LO) set_sides();
      wait_until(*(LAS u64*)(L + S_TML) + DLV(D_A1));
    } else {
      u64 t_ml = 0;
      if (LY == 0 && LO) vote();
      if (LO && w == 0) {
        v2u ml = ldx2(rws, o_mg, SM);
        bool bad = max(ml.x, ml.y) == SENT;
        REPOLL(34 + SO / 2, bad, ml = ldx2(rws, o_mg, SM), max(ml.x, ml.y) == SENT);
        t_ml = __builtin_amdgcn_s_memrealtime();
        if (l == 0) *(LAS u64*)(L + S_TML) = t_ml;
      }
      if (LY == 1) { BAR(); t_ml = *(LAS u64*)(L + S_TML); }
      STAMP(5 + SO);
      if (LY == 0) {
        if (LO) asm volatile("s_waitcnt vmcnt(0)" ::: "memory");
        if (ROLE == 2) {
          const rsrc_t rw2 = mkrs(p.w2, DM * FF * 2);
          const u32 o = (w < 7 && 128 * wa + 2 * l < DM) ? (224 * g + 32 * w) * (DM * 2) + wa * 256 + l * 4 : OOB;
#pragma unroll
          for (int i = 0; i < 32; ++i) w2_raw[i] = __builtin_amdgcn_raw_buffer_load_b32(rw2, o + i * (DM * 2), 0, NTW);
          SB();
          BAR();
        } else {
          if (!LO) {
            const int tt = t - 256;
            load_wx(WFE, 7);
            SB();
#pragma unroll
            for (int i = 0; i < 7; ++i) { const int idx = tt + 256 * i; *(LAS v4u*)(L + S_WX + ((idx ^ ((idx >> 5) & 7)) * 16)) = wx_raw[i]; }
          }
          BAR();
          v2u d[8];
#pragma unroll
          for (int jj = 0; jj < 8; ++jj) d[jj] = *(LAS v2u*)(L + S_WX + (((32 * lc + 4 * jj + (w >> 1)) ^ (lc & 7)) * 16) + (w & 1) * 8);
#pragma unroll
          for (int q = 0; q < 2; ++q)
#pragma unroll
            for (int i = 0; i < 4; ++i) { wx[2 * q][i] = __builtin_amdgcn_perm(d[2 * i + 1][q], d[2 * i][q], 0x05040100u); wx[2 * q + 1][i] = __builtin_amdgcn_perm(d[2 * i + 1][q], d[2 * i][q], 0x07060302u); }
          S13 = rows4(dot8(wx[0], onem4), dot8(wx[1], onem4), dot8(wx[2], onem4), dot8(wx[3], onem4));
        }
        t_ml = *(LAS u64*)(L + S_TML);
        if (!LO) set_sides();
      }
      wait_until(t_ml + DLV(LY == 0 ? D_DS0 : D_DS1));
      if (LY == 1) STAMP(49);
      v4u dv[4];
      auto dso = [&](int i) { return LY == 0 ? (u32)(i * 4 * 896) : sg + (i & 1) * 4096 + (i >> 1) * 16384; };
      if (LO) {
#pragma unroll
        for (int i = 0; i < 4; ++i) dv[i] = ldx4(rws, o_dg, dso(i));
      }
      if (LY == 0 && ROLE == 2) {
#pragma unroll
        for (int i = 0; i < 16; ++i) { wx[i >> 2][i & 3] = __builtin_amdgcn_perm(w2_raw[2 * i + 1], w2_raw[2 * i], 0x05040100u);
                                       wx[4 + (i >> 2)][i & 3] = __builtin_amdgcn_perm(w2_raw[2 * i + 1], w2_raw[2 * i], 0x07060302u); }
      }
      if (LO) {
        auto dbad = [&]() { return max(max(vmax(dv[0]), vmax(dv[1])), max(vmax(dv[2]), vmax(dv[3]))) == SENT; };
        bool bad = dbad();
        REPOLL(35 + SO / 2, bad, (dv[0] = ldx4(rws, o_dg, dso(0)), dv[1] = ldx4(rws, o_dg, dso(1)), dv[2] = ldx4(rws, o_dg, dso(2)), dv[3] = ldx4(rws, o_dg, dso(3))), dbad());
        STAMP(7 + SO);
        f4 x1 = {0.f, 0.f, 0.f, 0.f};
#pragma unroll
        for (int i = 0; i < 4; ++i) x1 = mfma(selm, dv[i], x1);
        if (a_xw < DM) {
          if (ROLE == 1) *(LAS v2u*)(L + S_XB + a_xw * 2) = v2u{cvtpk(x1[0], x1[1]), cvtpk(x1[2], x1[3])};
          else *(LAS f4*)(L + S_X1 + a_xw * 4) = x1;
        }
      }
      if constexpr (ROLE == 1) {
        BAR();
        const v4u xb = *(LAS v4u*)(L + S_XB + l * 16);
        float mu, r; ln_stats(xb, mu, r);
        float y = (rows4(dot8(xb, wx[0]), dot8(xb, wx[1]), dot8(xb, wx[2]), dot8(xb, wx[3])) - mu * S13) * r;
        if (!isup) y *= __builtin_amdgcn_rcpf(1.f + __builtin_amdgcn_exp2f(-1.4426950408889634f * y));
        auto s = __builtin_amdgcn_permlane16_swap(__float_as_uint(y), __float_as_uint(y), false, false);
        const u32 pk = cvtpk(__uint_as_float(s[0]), __uint_as_float(s[1]));
        auto s2 = __builtin_amdgcn_permlane32_swap(pk, pk, false, false);
        stx2(v2u{s2[0], s2[1]}, rws, o_pub0, SH + sg);
        STAMP(8 + SO);
        if (LY == 1) return;
        DLY(D_A2);
      } else {
        BAR();
        u32 sfin0 = 0, sfin1 = 0;
        if (LY == 1) {
#pragma unroll
          for (int r = 0; r < 4; ++r) if (r == wa) { sfin0 = srep[2 * r]; sfin1 = srep[2 * r + 1]; }
        }
        DLY(LY == 0 ? D_H0 : D_H1);
        u32 hg = ldx1(rws, o_gat, SH + sg), hu = ldx1(rws, o_gat, SH + sg + LH_UP);
        bool bad2 = max(hg, hu) == SENT;
        REPOLL(36 + SO / 2, bad2, (hg = ldx1(rws, o_gat, SH + sg), hu = ldx1(rws, o_gat, SH + sg + LH_UP)), max(hg, hu) == SENT);
        STAMP(8 + SO);
        const u32 hp = cvtpk(bflo(hg) * bflo(hu), bfhi(hg) * bfhi(hu));
        f2 acc = {0.f, 0.f};
        static_for<16>([&](auto I) { constexpr int i = decltype(I)::value;
          const u32 hb = (u32)__builtin_amdgcn_mov_dpp((int)hp, 0x150 + i, 0xf, 0xf, true);
          acc.x = dot2(hb, wx[i >> 2][i & 3], acc.x); acc.y = dot2(hb, wx[4 + (i >> 2)][i & 3], acc.y); });
        *(LAS f2*)(L + S_PART + w * 512 + l * 8) = acc;
        BAR();
        if (LO && w == 0) {
          f2 s = {0.f, 0.f};
          if (g == 0) s = *(LAS f2*)(L + S_X1 + min(128 * wa + 2 * l, DM - 2) * 4);
#pragma unroll
          for (int i = 0; i < 7; ++i) s += *(LAS f2*)(L + S_PART + i * 512 + l * 8);
          const u32 pk = cvtpk(s.x, s.y);
          if (LY == 0) {
#pragma unroll
            for (int r = 0; r < 8; ++r) stx1(pk, rws, o_pub0, srep[r]);
          } else {
            stx1(pk, rws, o_pub0, SA + sfin0); stx1(pk, rws, o_pub0, SA + sfin1);
          }
        }
        STAMP(9 + SO);
        if (LY == 1) {
          if (LO && w == 0 && j == 28 && g < 7) {
            DLY(D_F);
            const u32 o = n < 8 ? two(L_A + g * LA_REP + u * 1024 + g * 128 + n * 16) : OOB;
            v4u f0 = ldx4(rws, o, SA + sg), f1 = ldx4(rws, o, SA + sg + 4096);
            bool bad3 = max(vmax(f0), vmax(f1)) == SENT;
            REPOLL(45, bad3, (f0 = ldx4(rws, o, SA + sg), f1 = ldx4(rws, o, SA + sg + 4096)), max(vmax(f0), vmax(f1)) == SENT);
            const f4 y = mfma(selm, f1, mfma(selm, f0, f4{0.f, 0.f, 0.f, 0.f}));
            if (u < 2 && n < 8) *(v2u*)(p.out + 64 * g + 8 * n + 4 * u) = v2u{cvtpk(y[0], y[1]), cvtpk(y[2], y[3])};
            if (g == 0 && l == 0) stx1(T0, rws, WS_EP, 0);
            STAMP(10 + SO);
          }
          return;
        }
        DLY(D_A3);
      }
    }
    {
      if (LO) {
        v4u a0 = ldx4(rws, o_ag, sg), a1 = ldx4(rws, o_ag, sg + 4096);
        bool bad = max(vmax(a0), vmax(a1)) == SENT;
        REPOLL(37, bad, (a0 = ldx4(rws, o_ag, sg), a1 = ldx4(rws, o_ag, sg + 4096)), max(vmax(a0), vmax(a1)) == SENT);
        STAMP(10 + SO);
        const f4 x2 = mfma(selm, a1, mfma(selm, a0, f4{0.f, 0.f, 0.f, 0.f}));
        if (a_xw < DM) *(LAS v2u*)(L + S_XB + a_xw * 2) = v2u{cvtpk(x2[0], x2[1]), cvtpk(x2[2], x2[3])};
      }
      BAR();
      if (LO) {
        v4u xb = *(LAS v4u*)(L + S_XB + l * 16);
        const v2u qv = q_phase(xb);
        stx2(qv, rws, o_qs1, sg);
        STAMP(17);
        DLY(D_B1);
      }
    }
  };
  layer(std::integral_constant<int, 0>{});
  layer(std::integral_constant<int, 1>{});
  STAMP_FLUSH();
  asm volatile("; end of program %0 %1" :: "n"(ROLE), "n"(LO ? 1 : 0));
}

__global__ void __launch_bounds__(NT, 2) mega(Params p) {
  extern __shared__ __attribute__((aligned(16))) unsigned char lds_[];
  LAS unsigned char* L = (LAS unsigned char*)lds_;
  const int c = blockIdx.x, t = threadIdx.x, w = __builtin_amdgcn_readfirstlane(t >> 6), j = c >> 3;
  u64 code; u32 len;
  asm volatile("s_getpc_b64 %0\n.Lmk_pc:\n\ts_mov_b32 %1, " FUNC_END "-.Lmk_pc" : "=s"(code), "=s"(len));
  if (j < 14) { if (w < 4) program<0, true>(p, L, c, t, w, code, len); else program<0, false>(p, L, c, t, w, code, len); }
  else if (j < 28) { if (w < 4) program<1, true>(p, L, c, t, w, code, len); else program<1, false>(p, L, c, t, w, code, len); }
  else { if (w < 4) program<2, true>(p, L, c, t, w, code, len); else program<2, false>(p, L, c, t, w, code, len); }
}
__device__ __attribute__((noinline, used)) void code_end() { asm volatile("mk_code_end:"); }
}

extern "C" void kernel_launch(void* const* d_in, const int* in_sizes, int n_in, void* d_out, int out_size, void* d_ws, size_t ws_size, hipStream_t stream) {
  mk::Params p;
  p.x = (const mk::u16*)d_in[0]; p.k = (const mk::u16*)d_in[1]; p.v = (const mk::u16*)d_in[2];
  p.wq = (const mk::u16*)d_in[3]; p.wo = (const mk::u16*)d_in[4];
  p.w1 = (const mk::u16*)d_in[6]; p.w3 = (const mk::u16*)d_in[7]; p.w2 = (const mk::u16*)d_in[8];
  p.out = (mk::u16*)d_out; p.ws = (unsigned char*)d_ws;
  (void)hipFuncSetAttribute((const void*)mk::mega, hipFuncAttributeMaxDynamicSharedMemorySize, mk::LDS_BYTES);
  hipLaunchKernelGGL(mk::mega, dim3(256), dim3(mk::NT), mk::LDS_BYTES, stream, p);
}
```

```cpp
#include <hip/hip_runtime.h>
#include <stdint.h>
#include <type_traits>

namespace mk {
typedef unsigned u32; typedef unsigned short u16; typedef unsigned long long u64;
typedef u32 v4u __attribute__((ext_vector_type(4)));
typedef u32 v2u __attribute__((ext_vector_type(2)));
typedef float f2 __attribute__((ext_vector_type(2)));
typedef float f4 __attribute__((ext_vector_type(4)));
typedef short s4v __attribute__((ext_vector_type(4)));
typedef __bf16 bf2 __attribute__((ext_vector_type(2)));
typedef __bf16 bf8 __attribute__((ext_vector_type(8)));
#define LAS __attribute__((address_space(3)))
#define DEV __device__ __forceinline__

constexpr int DM = 448, SK = 8192, DH = 256, WIN = 2048, FF = 1792, NQ = 4096;
constexpr int NT = 512;
constexpr int WS_EP = 0;
constexpr int WS_RDY = 4096;
constexpr int W_Q0 = 8192, WQ0_GROUP = 8192;
constexpr int W_C0 = W_Q0 + 8 * WQ0_GROUP;
constexpr int W_ML0 = W_C0 + 262144;
constexpr int W_D0 = W_ML0 + 65536, WD0_REP = 16384;
constexpr int W_TWO = (W_D0 + 8 * WD0_REP + 16383) / 16384 * 16384;
__host__ __device__ constexpr int two(int o) { return W_TWO + ((o >> 13) << 14) + (o & 8191); }
constexpr int L_Q1 = 0, LQ1_GROUP = 4096;
constexpr int L_C1 = L_Q1 + 8 * LQ1_GROUP;
constexpr int L_ML1 = L_C1 + 262144;
constexpr int L_D1 = L_ML1 + 8192, LD1_REP = 16384;
constexpr int L_H = L_D1 + 8 * LD1_REP, LH_LAYER = 8192, LH_UP = 4096;
constexpr int L_A = L_H + 2 * LH_LAYER, LA_REP = 8192, LA_LAYER = 8 * LA_REP;
constexpr int L_PR = L_A + 2 * LA_LAYER;
constexpr int WS_BYTES = W_TWO + 2 * (L_PR + 131072);
static_assert(WS_BYTES <= (2 << 20), "one 2-MB page");
constexpr int WS_STAMP = 4 << 20;
constexpr u32 SENT = 0xFFFFFFFFu;
constexpr u32 OOB = 0x80000000u;

constexpr int S_WQ = 0;
constexpr int S_K = S_WQ + 16384;
constexpr int S_V = S_K + 32768;
constexpr int S_WX = S_V + 32768;
constexpr int S_Q = S_WX + 32768;
constexpr int S_SC = S_Q + 1280;
constexpr int S_P = S_SC + 640;
constexpr int S_E = S_P + 8 * 320;
constexpr int S_PART = S_E + 8 * 128;
constexpr int S_XB = S_PART + 4096;
constexpr int S_X1 = S_XB + 1024;
constexpr int S_HB = S_X1 + 1792;
constexpr int S_CNT = S_HB + 32;
constexpr int S_EP = S_HB + 48;
constexpr int S_TML = S_HB + 64;
constexpr int S_END = S_HB + 80;
constexpr int LDS_BYTES = (S_END + 255) / 256 * 256;
static_assert(LDS_BYTES > 81920 && LDS_BYTES <= 163840, "LDS must pin one workgroup per CU");

struct Params {
  const u16 *x, *k, *v, *wq, *wo, *w1, *w3, *w2;
  u16* out;
  unsigned char* ws;
};

#define SPIN_CAP (1u << 18)
#define BACKOFF 1
#define NTW 2
#define WARM 1
#define WARM_AUX 2
enum { D_B0, D_TB0, D_B1, D_C0, D_C1, D_DS0, D_DS1, D_H0, D_H1, D_A1, D_A2, D_A3, D_F, D_C0B, D_DSH, D_N };
#define DL_DEFAULT {20, 340, 18, 24, 18, 60, 62, 54, 48, 110, 60, 12, 8, 2, 30}
#define PROBE_THR 110
#define FUNC_END "mk_code_end"
#define VSLEEP 0
#define CODEPF 1
#define WOE 8
#define WFE 4
#define DLV(k) dl_default[k]
constexpr int dl_default[D_N] = DL_DEFAULT;

DEV float bflo(u32 v) { return __uint_as_float(v << 16); }
DEV u32 cvtpk(float a, float b) { f2 v = {a, b}; bf2 r = __builtin_convertvector(v, bf2); return __builtin_bit_cast(u32, r); }
DEV float dot2(u32 a, u32 b, float c) { return __builtin_amdgcn_fdot2_f32_bf16(__builtin_bit_cast(bf2, a), __builtin_bit_cast(bf2, b), c, false); }
DEV f4 mfma(v4u a, v4u b, f4 c) { return __builtin_amdgcn_mfma_f32_16x16x32_bf16(__builtin_bit_cast(bf8, a), __builtin_bit_cast(bf8, b), c, 0, 0, 0); }
template <int C> DEV float dpp(float x) { return __builtin_bit_cast(float, __builtin_amdgcn_mov_dpp(__builtin_bit_cast(int, x), C, 0xf, 0xf, true)); }
constexpr int ROR8 = 0x128, ROR4 = 0x124, ROR2 = 0x122, ROR1 = 0x121, QX1 = 0xB1, QX2 = 0x4E, HMIR = 0x141;
DEV float sw32_add(float a, float b) { auto s = __builtin_amdgcn_permlane32_swap(__float_as_uint(a), __float_as_uint(b), false, false); return __uint_as_float(s[0]) + __uint_as_float(s[1]); }
DEV float sw16_add(float a, float b) { auto s = __builtin_amdgcn_permlane16_swap(__float_as_uint(a), __float_as_uint(b), false, false); return __uint_as_float(s[0]) + __uint_as_float(s[1]); }
DEV float vmaxf(float a, float b) { return __builtin_amdgcn_fmed3f(a, b, __builtin_inff()); }
DEV float sw32_max(float a, float b) { auto s = __builtin_amdgcn_permlane32_swap(__float_as_uint(a), __float_as_uint(b), false, false); return vmaxf(__uint_as_float(s[0]), __uint_as_float(s[1])); }
DEV float sw16_max(float a, float b) { auto s = __builtin_amdgcn_permlane16_swap(__float_as_uint(a), __float_as_uint(b), false, false); return vmaxf(__uint_as_float(s[0]), __uint_as_float(s[1])); }
DEV float row_sum(float z) { z += dpp<ROR8>(z); z += dpp<ROR4>(z); z += dpp<ROR2>(z); z += dpp<ROR1>(z); return z; }
DEV float row_max(float z) { z = vmaxf(z, dpp<ROR8>(z)); z = vmaxf(z, dpp<ROR4>(z)); z = vmaxf(z, dpp<ROR2>(z)); z = vmaxf(z, dpp<ROR1>(z)); return z; }
DEV float sum4(float a, float b, float c, float d) { return row_sum(sw16_add(sw32_add(a, c), sw32_add(b, d))); }
DEV float rdl(float v, int l) { return __uint_as_float(__builtin_amdgcn_readlane(__float_as_uint(v), l)); }
DEV void pin(u32& v) { asm volatile("" : "+v"(v)); }
DEV void pin(v4u& v) { asm volatile("" : "+v"(v)); }
#define SB() __builtin_amdgcn_sched_barrier(0)
#define BAR() do { asm volatile("s_waitcnt lgkmcnt(0)" ::: "memory"); __builtin_amdgcn_s_barrier(); asm volatile("" ::: "memory"); } while (0)
DEV void wait_until(u64 t) { asm volatile("" ::: "memory"); while (__builtin_amdgcn_s_memrealtime() < t) {} asm volatile("" ::: "memory"); SB(); }
#define DLY(k) wait_until(__builtin_amdgcn_s_memrealtime() + DLV(k))
#define CNT(i)
#define STAMP(i)
#define STAMP_FLUSH()
#define REPOLL(id, bad, LOADS, BAD) do { if (__builtin_expect(__any(bad), 0)) { for (u32 sp_ = 0; sp_ < SPIN_CAP; ++sp_) { asm volatile("" ::: "memory"); CNT(id); \
    if (BACKOFF) __builtin_amdgcn_s_sleep(BACKOFF); LOADS; if (!__any(BAD)) break; } } asm volatile("" ::: "memory"); } while (0)

typedef __amdgpu_buffer_rsrc_t rsrc_t;
DEV rsrc_t mkrs(const void* p, u32 bytes) { return __builtin_amdgcn_make_buffer_rsrc((void*)p, 0, bytes, 0x00020000); }
DEV v4u ldw(rsrc_t r, u32 off) { return __builtin_amdgcn_raw_buffer_load_b128(r, off, 0, NTW); }
DEV v4u ldx4(rsrc_t r, u32 off, u32 so) { return __builtin_amdgcn_raw_buffer_load_b128(r, off, so, 16); }
DEV v2u ldx2(rsrc_t r, u32 off, u32 so) { return __builtin_amdgcn_raw_buffer_load_b64(r, off, so, 16); }
DEV u32 ldx1(rsrc_t r, u32 off, u32 so) { return __builtin_amdgcn_raw_buffer_load_b32(r, off, so, 16); }
DEV void stx4(v4u v, rsrc_t r, u32 off, u32 so) { __builtin_amdgcn_raw_buffer_store_b128(v, r, off, so, 16); }
DEV void stx2(v2u v, rsrc_t r, u32 off, u32 so) { __builtin_amdgcn_raw_buffer_store_b64(v, r, off, so, 16); }
DEV void stx1(u32 v, rsrc_t r, u32 off, u32 so) { __builtin_amdgcn_raw_buffer_store_b32(v, r, off, so, 16); }
DEV u32 vmax(v4u v) { return max(max(max(v.x, v.y), v.z), v.w); }
constexpr u32 ONE = 0x3F803F80u;

DEV void read_cols(LAS unsigned char* base, int lc, int w, v4u& wa, v4u& wb) {
  u32 d[8];
#pragma unroll
  for (int jj = 0; jj < 8; ++jj) d[jj] = *(LAS u32*)(base + (((16 * lc + 2 * jj + (w >> 2)) ^ (lc & 7)) * 16) + (w & 3) * 4);
#pragma unroll
  for (int i = 0; i < 4; ++i) { wa[i] = __builtin_amdgcn_perm(d[2 * i + 1], d[2 * i], 0x05040100u); wb[i] = __builtin_amdgcn_perm(d[2 * i + 1], d[2 * i], 0x07060302u); }
}
DEV float dot8(const v4u x, const v4u wv) { float a = 0.f;
#pragma unroll
  for (int i = 0; i < 4; ++i) a = dot2(x[i], wv[i], a);
  return a; }
DEV float rows4(float a, float b, float c, float d) { return row_sum(sw16_add(sw32_add(a, c), sw32_add(b, d))); }
DEV void ln_stats(const v4u x, float& mu, float& r) {
  float st = sw32_add(dot8(x, v4u{ONE, ONE, ONE, ONE}), dot8(x, x)); st = row_sum(sw16_add(st, st));
  mu = rdl(st, 0) * (1.f / DM);
  r = __builtin_amdgcn_rsqf(fmaxf(fmaf(rdl(st, 32), 1.f / DM, -mu * mu), 0.f) + 1e-5f);
}
DEV float reduce_rows(const float (&v8)[8], int l) {
  float v4_[4], v2_[2];
#pragma unroll
  for (int i = 0; i < 4; ++i) v4_[i] = sw32_add(v8[i], v8[i + 4]);
#pragma unroll
  for (int i = 0; i < 2; ++i) v2_[i] = sw16_add(v4_[i], v4_[i + 2]);
  const bool k8 = l & 8;
  return (k8 ? v2_[1] : v2_[0]) + dpp<ROR8>(k8 ? v2_[0] : v2_[1]);
}
template <int N, int I = 0, class F> DEV void static_for(F&& f) { if constexpr (I < N) { f(std::integral_constant<int, I>{}); static_for<N, I + 1>(f); } }
DEV float split_weights(const v2u ml, LAS unsigned char* E, int l) {
  const float mv = __uint_as_float(ml.x);
  float M = row_max(mv); M = sw16_max(M, M);
  const float e = __builtin_amdgcn_exp2f(mv - M);
  *(LAS float*)(E + (l & 31) * 4) = e;
  return e;
}
DEV float split_norm(float e, const v2u ml) { float Ls = row_sum(e * __uint_as_float(ml.y)); return sw16_add(Ls, Ls); }
DEV v4u sel_matrix(int n) { v4u a = {0u, 0u, 0u, 0u};
#pragma unroll
  for (int i = 0; i < 4; ++i) a[i] = (n >> 1) == i ? ((n & 1) ? 0x3F800000u : 0x00003F80u) : 0u;
  return a; }
DEV float bfhi(u32 v) { return __uint_as_float(v & 0xFFFF0000u); }

struct Pre {
  v4u xp, ep, wq_raw[4];
  v4u kv_raw[8];
  u64 t_start;
  u32 code_pf;
};
template <bool LO>
DEV void prologue(const Params& p, LAS unsigned char* L, const int c, const int t, const int w, const u64 code, const u32 code_len, Pre& s) {
  const int g = c & 7, j = c >> 3, l = t & 63;
  STAMP(0);
  s.t_start = LO ? __builtin_amdgcn_s_memrealtime() : 0;
  if constexpr (LO) {
    s.code_pf = 0;
    if (CODEPF) s.code_pf = __builtin_amdgcn_raw_buffer_load_b32(mkrs((const void*)code, code_len & ~3u), w == 3 ? (j + 32 * l) * 128 : OOB, 0, 0);
    SB();
  }
  const u32 kvbase = ((u32)g * SK + (SK - WIN) + j * 64) * (DH * 2);
  if constexpr (LO) {
    const rsrc_t rws = mkrs(p.ws, WS_BYTES);
    const rsrc_t rwq = mkrs(p.wq, DM * NQ * 2);
    const u32 o = (112 * (j >> 3) + (t >> 3)) * (NQ * 2) + (g * 512 + (j & 7) * 64) * 2 + (t & 7) * 16;
#pragma unroll
    for (int i = 0; i < 4; ++i) s.wq_raw[i] = ldw(rwq, (i < 3 || t < 128) ? o + i * 32 * (NQ * 2) : OOB);
    s.xp = __builtin_amdgcn_raw_buffer_load_b128(mkrs(p.x, DM * 2), w == 0 ? l * 16 : OOB, 0, 0);
    s.ep = ldx4(rws, (w == 0 && l == 0) ? WS_EP : OOB, 0);
    SB();
    const int hh = j >= 7, wa = j - 28;
    u32 oc = OOB;
    if (w == 0) oc = W_C0 + g * 32768 + j * 1024 + l * 16;
    if (w == 1 && j < 14) oc = W_D0 + (l >> 3) * WD0_REP + (2 * g + hh) * 896 + (j - 7 * hh) * 128 + (l & 7) * 16;
    if (w == 2 && l < 8) oc = two(L_PR + c * 512 + (l >> 1) * 128) + (l & 1) * 8192;
    if (w == 3 && l < 2) oc = W_ML0 + ((2 * g + l) * 32 + j) * 128;
    stx4(v4u{SENT, SENT, SENT, SENT}, rws, oc, 0);
    oc = OOB;
    if (j >= 14 && j < 28 && w == 0 && l < 8) oc = two(L_H + (j >= 21 ? LH_UP : 0) + (7 * g + (j - (j >= 21 ? 21 : 14))) * 64 + (l & 3) * 16) + (l >> 2) * 8192;
    if (j >= 28 && (l & 15) + 16 * wa < 56) oc = two(L_A + (2 * w + (l >> 5)) * LA_REP + g * 1024 + wa * 256 + (l & 15) * 16) + ((l >> 4) & 1) * 8192;
    stx4(v4u{SENT, SENT, SENT, SENT}, rws, oc, 0);
    SB();
    const rsrc_t rk = mkrs(p.k, 8u * SK * DH * 2);
#pragma unroll
    for (int i = 0; i < 8; ++i) s.kv_raw[i] = ldw(rk, kvbase + (t + 256 * i) * 16);
    SB();
    if (t == 0) *(LAS v4u*)(L + S_CNT) = v4u{0u, 0u, 0u, 0u};
    asm volatile("s_waitcnt lgkmcnt(0)" ::: "memory");
  } else {
    if (VSLEEP) __builtin_amdgcn_s_sleep(VSLEEP);
    const rsrc_t rv = mkrs(p.v, 8u * SK * DH * 2);
#pragma unroll
    for (int i = 0; i < 8; ++i) s.kv_raw[i] = ldw(rv, kvbase + (t - 256 + 256 * i) * 16);
    STAMP(29);
  }
  SB();
  __builtin_amdgcn_s_barrier();
  SB();
  STAMP(12);
}

template <int ROLE, bool LO>
DEV void program(const Params& p, LAS unsigned char* L, const int c, const int t, const int w, Pre& pre) {
  const int g = c & 7, j = c >> 3, l = t & 63, u = l >> 4, n = l & 15;
  asm volatile("; start of program %0 %1" :: "n"(ROLE), "n"(LO ? 1 : 0) : "memory");
  const u64 t_start = pre.t_start;
  const rsrc_t rws = mkrs(p.ws, WS_BYTES);
  const int hh = j >= 7, h = 2 * g + hh, cb = j - 7 * hh;
  const bool isup = j >= 21;
  const int ub = 7 * g + (j - (isup ? 21 : 14));
  const int wa = j - 28;
  const int kq = j >> 3, cq = j & 7;
  v4u xp = pre.xp; const v4u ep = pre.ep;
  v4u (&wq_raw)[4] = pre.wq_raw; v4u (&k_raw)[8] = pre.kv_raw; v4u (&v_raw)[8] = pre.kv_raw;
  const u32 code_pf = pre.code_pf;
  v4u wx_raw[8];
  STAMP(13);
  u32 o_qs0 = n == 0 ? W_Q0 + g * WQ0_GROUP + kq * 2048 + (16 * cq + 4 * w + u) * 16 : OOB;
  u32 o_qs1 = n == 0 ? two(L_Q1 + g * LQ1_GROUP + kq * 1024 + (64 * cq + 16 * w + 4 * u) * 2) : OOB;
  u32 o_qg0 = W_Q0 + g * WQ0_GROUP + (l >> 5) * 4096 + (32 * w + (l & 31)) * 16;
  u32 o_qg1 = two(L_Q1 + g * LQ1_GROUP + u * 1024 + (128 * w + 8 * n) * 2);
  const int cofs = (2 * g + n) * 16384 + (j >> 4) * 8192 + w * 1024 + (u * 16 + (j & 15)) * 16;
  u32 o_cs0 = n < 2 ? W_C0 + cofs : OOB, o_cs1 = n < 2 ? two(L_C1 + cofs) : OOB;
  u32 o_ms0 = (l & 31) == 0 ? W_ML0 + ((2 * g + (l >> 5)) * 32 + j) * 128 : OOB, o_ms1 = (l & 31) == 0 ? two(L_ML1 + ((2 * g + (l >> 5)) * 32 + j) * 8) : OOB;
  const int hx = ROLE == 0 ? h : 2 * g;
  const int gofs = hx * 16384 + (w & 1) * 8192 + (w >> 1) * 2048 + l * 16;
  u32 o_cg0 = W_C0 + gofs, o_cg1 = two(L_C1 + gofs);
  u32 o_mg0 = W_ML0 + (hx * 32 + (l & 31)) * 128, o_mg1 = two(L_ML1 + (hx * 32 + (l & 31)) * 8);
  u32 o_pub0 = OOB, o_pub1 = OOB, o_gat = OOB;
  if (ROLE == 0) { o_pub0 = (l & 1) == 0 ? W_D0 + h * 896 + cb * 128 + (l >> 1) * 4 : OOB;
                   o_pub1 = (l & 1) == 0 ? two(L_D1 + h * 1024 + cb * 128 + (l >> 1) * 4) : OOB; }
  else if (ROLE == 1) o_pub0 = l == 0 ? two(L_H + (isup ? LH_UP : 0) + ub * 64 + w * 8) : OOB;
  else { o_pub0 = 128 * wa + 2 * l < DM ? two(L_A + g * 1024 + wa * 256 + l * 4) : OOB;
         o_gat = w < 7 ? two(L_H + (224 * g + 32 * w) * 2 + n * 4) : OOB; }
  const bool cin = LO && 128 * w + 8 * n < DM;
  u32 o_dg0 = cin ? W_D0 + g * WD0_REP + u * 896 + w * 256 + n * 16 : OOB;
  u32 o_dg1 = cin ? two(L_D1 + g * LD1_REP + u * 1024 + w * 256 + n * 16) : OOB;
  u32 o_ag = cin ? two(L_A + g * LA_REP + u * 1024 + w * 256 + n * 16) : OOB;
  u32 a_xw = (cin && u < 2) ? (128 * w + 8 * n + 4 * u) : 0xFFFF;
  v4u selm = sel_matrix(n);
  pin(o_cs0); pin(o_cs1); pin(o_mg0); pin(o_mg1); pin(o_pub0);
  if (LO) { pin(o_ag); pin(a_xw); pin(selm); pin(o_qs0); pin(o_qs1); pin(o_qg0); pin(o_qg1); pin(o_ms0); pin(o_ms1); }
  if (ROLE == 0) { pin(o_cg0); pin(o_cg1); pin(o_pub1); }
  if (ROLE == 2) pin(o_gat);
  if (ROLE != 0 && LO) { pin(o_dg0); pin(o_dg1); }
  const int lc = min(l, 55);
  const u32 onem = l < 56 ? ONE : 0u;
  const v4u onem4 = {onem, onem, onem, onem};
  SB();
  u32 T0 = 0, T1 = 0, R0 = 0, R1 = 0, R2 = 0, R3 = 0;
  auto lo_sync = [&](u32 target) {
    asm volatile("s_waitcnt lgkmcnt(0)" ::: "memory");
    if (l == 0) __hip_atomic_fetch_add((LAS u32*)(L + S_CNT), 1u, __ATOMIC_RELAXED, __HIP_MEMORY_SCOPE_WORKGROUP);
    while (*(volatile LAS u32*)(L + S_CNT) < target) {}
    asm volatile("" ::: "memory");
  };
  v4u kf[8];
  v4u wqf[4];
  f4 Sq;
  auto q_phase = [&](const v4u xs) {
    f4 acc = {0.f, 0.f, 0.f, 0.f};
#pragma unroll
    for (int s = 0; s < 4; ++s) acc = mfma(wqf[s], *(LAS v4u*)(L + S_XB + (112 * kq + 32 * s + 8 * u) * 2), acc);
    float mu, r; ln_stats(xs, mu, r);
    return v2u{cvtpk((acc[0] - mu * Sq[0]) * r, (acc[1] - mu * Sq[1]) * r), cvtpk((acc[2] - mu * Sq[2]) * r, (acc[3] - mu * Sq[3]) * r)};
  };
  if constexpr (LO) {
#pragma unroll
    for (int i = 0; i < 4; ++i) *(LAS v4u*)(L + S_WQ + (t + 256 * i) * 16) = wq_raw[i];
    if (w == 0) { *(LAS v4u*)(L + S_XB + l * 16) = xp; if (l == 0) *(LAS v4u*)(L + S_EP) = ep; }
    lo_sync(4);
    STAMP(11);
    xp = *(LAS v4u*)(L + S_XB + l * 16);
    { const v4u e = *(LAS v4u*)(L + S_EP);
      T0 = __builtin_amdgcn_readfirstlane(e.x) + 1u; T1 = __builtin_amdgcn_readfirstlane(e.y); const u32 T2 = __builtin_amdgcn_readfirstlane(e.z);
      R0 = T0 << 3; R1 = (T0 * 0x9E3779B1u ^ T1) & ~7u; R2 = (T0 * 0x85EBCA77u ^ T2) & ~7u; R3 = (T0 * 0xC2B2AE3Du ^ T1 ^ (T2 >> 7)) & ~7u; }
#pragma unroll
    for (int s = 0; s < 4; ++s) {
      LAS unsigned char* a = L + S_WQ + (32 * s + 8 * u + (n >> 2)) * 128 + w * 32 + (n & 3) * 8;
      const v2u h0 = __builtin_bit_cast(v2u, __builtin_amdgcn_ds_read_tr16_b64_v4i16((LAS s4v*)a));
      const v2u h1 = __builtin_bit_cast(v2u, __builtin_amdgcn_ds_read_tr16_b64_v4i16((LAS s4v*)(a + 512)));
      wqf[s] = v4u{h0.x, h0.y, h1.x, h1.y};
    }
    Sq = f4{0.f, 0.f, 0.f, 0.f};
#pragma unroll
    for (int s = 0; s < 4; ++s) Sq = mfma(wqf[s], v4u{ONE, ONE, ONE, ONE}, Sq);
    { const v2u qv = q_phase(xp);
      asm volatile("s_waitcnt vmcnt(8)" ::: "memory");
      stx4(v4u{qv.x, qv.y, T0, T1}, rws, o_qs0, 0); }
    STAMP(1);
    if (CODEPF) { u32 tmp_ = code_pf; pin(tmp_); }
#pragma unroll
    for (int i = 0; i < 8; ++i) { const int idx = t + 256 * i, key = idx >> 5; *(LAS v4u*)(L + S_K + key * 512 + (((idx & 31) ^ (key & 15)) * 16)) = k_raw[i]; }
    lo_sync(8);
#pragma unroll
    for (int s = 0; s < 8; ++s) kf[s] = *(LAS v4u*)(L + S_K + (16 * w + n) * 512 + (((4 * s + u) ^ n) * 16));
    {
      const u32 first = (j ^ w) & 1, oa = two(L_PR + c * 512 + w * 128) + first * 8192, ob = two(L_PR + c * 512 + w * 128) + (first ^ 1) * 8192;
      u32 va = ldx1(rws, l == 0 ? oa : OOB, 0), vb = ldx1(rws, l == 0 ? ob : OOB, 0);
      SB();
      asm volatile("s_waitcnt vmcnt(1)" : "+v"(va) :: "memory");
      const u64 t1 = __builtin_amdgcn_s_memtime();
      asm volatile("s_waitcnt lgkmcnt(0)\n\ts_waitcnt vmcnt(0)" : "+v"(vb) :: "memory");
      const u64 t2 = __builtin_amdgcn_s_memtime();

      u32 xcc; asm volatile("s_getreg_b32 %0, hwreg(HW_REG_XCC_ID, 0, 4)" : "=s"(xcc));
      const u32 die = (xcc >> 2) & 1;
      const u32 vt = (u32)(t2 - t1) > PROBE_THR ? ((first ^ die) ? 1u : 2u) : 0u;
      __builtin_amdgcn_raw_buffer_store_b32((w == 0 ? R0 : w == 1 ? R1 : w == 2 ? R2 : R3) | (die << 2) | vt, rws, l == 0 ? WS_RDY + c * 16 + w * 4 : OOB, 0, 16);
    }
    STAMP(14);
  }

  u32 sg = 0, srep[8];
  auto set_sides = [&]() {
    const v4u cw = *(LAS v4u*)(L + S_CNT);
    const u32 sm = __builtin_amdgcn_readfirstlane(((int)cw.z > 0 ? ~cw.w : cw.w) & 0xFFu);
    sg = ((sm >> g) & 1u) << 13;
#pragma unroll
    for (int r = 0; r < 8; ++r) srep[r] = r * (ROLE == 0 ? 2 * LD1_REP : 2 * LA_REP) + (((sm >> r) & 1u) << 13);
  };
  auto vote = [&]() {
    v4u rdy = ldx4(rws, WS_RDY + t * 16, 0);
    auto rbad = [&]() { return ((rdy.x & ~7u) != R0) | ((rdy.y & ~7u) != R1) | ((rdy.z & ~7u) != R2) | ((rdy.w & ~7u) != R3); };
    bool bad = rbad();
    REPOLL(33, bad, rdy = ldx4(rws, WS_RDY + t * 16, 0), rbad());
    const int votes = __popcll(__ballot(rdy.x & 1u)) + __popcll(__ballot(rdy.y & 1u)) + __popcll(__ballot(rdy.z & 1u)) + __popcll(__ballot(rdy.w & 1u))
                    - __popcll(__ballot(rdy.x & 2u)) - __popcll(__ballot(rdy.y & 2u)) - __popcll(__ballot(rdy.z & 2u)) - __popcll(__ballot(rdy.w & 2u));
    const u64 dies = __ballot(rdy.w & 4u);
    if (l == 0) { __hip_atomic_fetch_add((LAS u32*)(L + S_CNT + 8), (u32)votes, __ATOMIC_RELAXED, __HIP_MEMORY_SCOPE_WORKGROUP);
                  if (w == 0) *(LAS u32*)(L + S_CNT + 12) = (u32)dies & 0xFFu; }
    lo_sync(16);
    set_sides();
    if (ROLE != 0) {
      const int m = j - 14, ch = 2 * m + w;
      u32 oc = OOB;
      if (w < 2) oc = two(ch < 32 ? L_C1 + g * 32768 + ch * 1024 + l * 16 : L_Q1 + g * LQ1_GROUP + (ch - 32) * 1024 + l * 16);
      else if (w == 2) {
        if (m < 16) oc = two(L_D1 + g * LD1_REP + m * 1024 + l * 16);
        else if (m == 16) { if (l < 32) oc = two(L_ML1 + g * 512 + l * 16); }
        else if ((l & 31) < 28) oc = two(L_H + LH_LAYER + (l >> 5) * LH_UP + g * 448 + (l & 31) * 16);
      } else if (m < 8) oc = two(L_A + LA_LAYER + g * LA_REP + m * 1024 + l * 16);
      stx4(v4u{SENT, SENT, SENT, SENT}, rws, oc == OOB ? OOB : oc + sg, 0);
    }
  };

  v4u vf[2][2];
  float S13 = 0.f;
  v4u wx[8];
  u32 w2_raw[32];

  auto load_wx = [&](int i0, int i1) {
    const int tt = t - 256;
    if (ROLE == 0) {
      const rsrc_t rwo = mkrs(p.wo, DM * NQ * 2);
#pragma unroll
      for (int i = 0; i < 8; ++i) if (i >= i0 && i < i1) wx_raw[i] = ldw(rwo, (h * 256 + (tt >> 3) + 32 * i) * (DM * 2) + cb * 128 + (tt & 7) * 16);
    } else if (ROLE == 1) {
      const rsrc_t r1 = mkrs(isup ? p.w3 : p.w1, DM * FF * 2);
      const u32 o = (tt >> 2) * (FF * 2) + ub * 64 + (tt & 3) * 16;
#pragma unroll
      for (int i = 0; i < 7; ++i) if (i >= i0 && i < i1) wx_raw[i] = ldw(r1, o + i * 64 * (FF * 2));
    }
  };

  auto layer = [&](auto LC) {
    constexpr int LY = decltype(LC)::value;
    constexpr u32 SH = LY * 2 * LH_LAYER, SA = LY * 2 * LA_LAYER;
    constexpr int SO = 16 * LY;
    const u32 o_ms = LY == 0 ? o_ms0 : o_ms1, o_mg = LY == 0 ? o_mg0 : o_mg1, o_cs = LY == 0 ? o_cs0 : o_cs1, o_cg = LY == 0 ? o_cg0 : o_cg1, o_dg = LY == 0 ? o_dg0 : o_dg1;
    const u32 SM = LY == 0 ? 0u : sg, SC = SM;
    if (LO) {
      if (LY == 0) {
        DLY(D_B0);
        wait_until(t_start + DLV(D_TB0));
        auto qbad = [&](const v4u& q) { return (q.z != T0) | (q.w != T1); };
        v4u q0 = ldx4(rws, o_qg0, 0), q1 = ldx4(rws, o_qg0 + 2048, 0);
        SB();
        if (l == 0) __hip_atomic_fetch_add((LAS u32*)(L + S_CNT + 4), 1u, __ATOMIC_RELAXED, __HIP_MEMORY_SCOPE_WORKGROUP);
        SB();
        bool bad = qbad(q0) | qbad(q1);
        REPOLL(32, bad, (q0 = ldx4(rws, o_qg0, 0), q1 = ldx4(rws, o_qg0 + 2048, 0)), qbad(q0) | qbad(q1));
        const float c01 = sw32_add(bflo(q0.x) + bflo(q1.x), bflo(q0.y) + bflo(q1.y));
        const float c13 = sw32_add(bfhi(q0.x) + bfhi(q1.x), bfhi(q0.y) + bfhi(q1.y));
        *(LAS u32*)(L + S_Q + (w >> 1) * 576 + ((32 * w + (l & 31)) & 63) * 8 + (l >> 5) * 4) = cvtpk(c01, c13);
        lo_sync(12);
      } else {
        if (LY == 1) STAMP(50);
        v4u q0 = ldx4(rws, o_qg1, sg);
        bool bad = vmax(q0) == SENT;
        REPOLL(40, bad, q0 = ldx4(rws, o_qg1, sg), vmax(q0) == SENT);
        const f4 s = mfma(selm, q0, f4{0.f, 0.f, 0.f, 0.f});
        if (u < 2) *(LAS v2u*)(L + S_Q + (w >> 1) * 576 + ((128 * w + 8 * n + 4 * u) & 255) * 2) = v2u{cvtpk(s[0], s[1]), cvtpk(s[2], s[3])};
      }
    } else if (LY == 0) {
      if ((ROLE == 0 && WOE > 0) || (ROLE == 1 && WFE > 0)) {
        while (*(volatile LAS u32*)(L + S_CNT + 4) < 4u) __builtin_amdgcn_s_sleep(1);
        asm volatile("" ::: "memory");
        load_wx(0, ROLE == 0 ? WOE : WFE);
      }
      const int tt = t - 256;
      SB();
      STAMP(30);
#pragma unroll
      for (int i = 0; i < 8; ++i) { const int idx = tt + 256 * i, row = idx >> 5, gr = idx & 31; *(LAS v4u*)(L + S_V + row * 512 + ((gr ^ (4 * (row & 3))) * 16)) = v_raw[i]; }
      STAMP(31);
    }
    if (LY == 1) BAR();
    STAMP(2 + SO);
    if (LO) {
      LAS unsigned char* Q = L + S_Q;
      f4 acc = {0.f, 0.f, 0.f, 0.f};
#pragma unroll
      for (int s = 0; s < 8; ++s) { const v4u qf = *(LAS v4u*)(Q + (n & 1) * 576 + s * 64 + u * 16); acc = mfma(kf[s], qf, acc); }
      if (n < 2) *(LAS f4*)(L + S_SC + n * 272 + (16 * w + 4 * u) * 4) = acc;
    }
    BAR();
    STAMP(3 + SO);
    if (LY == 0) {
#pragma unroll
      for (int tl = 0; tl < 2; ++tl)
#pragma unroll
        for (int st = 0; st < 2; ++st) {
          const int key = 32 * st + 8 * u + (n >> 2), gr = 4 * w + (n & 3);
          LAS unsigned char* a = L + S_V + key * 512 + ((gr ^ (4 * (key & 3))) * 16) + tl * 8;
          const v2u h0 = __builtin_bit_cast(v2u, __builtin_amdgcn_ds_read_tr16_b64_v4i16((LAS s4v*)a));
          const v2u h1 = __builtin_bit_cast(v2u, __builtin_amdgcn_ds_read_tr16_b64_v4i16((LAS s4v*)(a + 2048)));
          vf[tl][st] = v4u{h0.x, h0.y, h1.x, h1.y};
        }
    }
    {
      const float s0 = *(LAS float*)(L + S_SC + l * 4), s1 = *(LAS float*)(L + S_SC + 272 + l * 4);
      constexpr float CE = 0.0625f * 1.4426950408889634f;
      float m = row_max(sw32_max(s0, s1));
      m = sw16_max(m, m) * CE;
      auto ms = __builtin_amdgcn_permlane32_swap(__float_as_uint(m), __float_as_uint(m), false, false);
      const float p0 = __builtin_amdgcn_exp2f(fmaf(s0, CE, -__uint_as_float(ms[0]))), p1 = __builtin_amdgcn_exp2f(fmaf(s1, CE, -__uint_as_float(ms[1])));
      const u32 pk = cvtpk(p0, p1);
      *(LAS u16*)(L + S_P + w * 320 + l * 2) = (u16)pk;
      *(LAS u16*)(L + S_P + w * 320 + 160 + l * 2) = (u16)(pk >> 16);
      const v4u pf0 = *(LAS v4u*)(L + S_P + w * 320 + (n & 1) * 160 + u * 16), pf1 = *(LAS v4u*)(L + S_P + w * 320 + (n & 1) * 160 + 64 + u * 16);
      f4 o0 = {0.f, 0.f, 0.f, 0.f}, o1 = o0;
      o0 = mfma(vf[0][0], pf0, o0); o1 = mfma(vf[1][0], pf0, o1);
      o0 = mfma(vf[0][1], pf1, o0); o1 = mfma(vf[1][1], pf1, o1);
      stx4(v4u{cvtpk(o0[0], o0[1]), cvtpk(o0[2], o0[3]), cvtpk(o1[0], o1[1]), cvtpk(o1[2], o1[3])}, rws, o_cs, SC);
      if (LO) {
        float ls = row_sum(sw32_add(p0, p1)); ls = sw16_add(ls, ls);
        stx2(v2u{__float_as_uint(m), __float_as_uint(ls)}, rws, w == 0 ? o_ms : OOB, SM);
      }
    }
    STAMP(4 + SO);
    if (!(LY == 0 && !LO && ROLE != 0)) DLY(LY == 0 ? D_C0 : D_C1);
    if constexpr (ROLE == 0) {
      float Ls = 1.f;
      if (LY == 0) {
        if (LO) {
          if (w == 0) {
            v2u m0 = ldx2(rws, o_mg, SM);
            bool badm = max(m0.x, m0.y) == SENT;
            REPOLL(38, badm, m0 = ldx2(rws, o_mg, SM), max(m0.x, m0.y) == SENT);
            Ls = split_norm(split_weights(m0, L + S_E, l), m0);
          }
          STAMP(53);
        } else {
          load_wx(WOE, 8);
          SB();
#pragma unroll
          for (int i = 0; i < 8; ++i) *(LAS v4u*)(L + S_WX + (t - 256 + 256 * i) * 16) = wx_raw[i];
        }
        BAR();
        STAMP(54);
        DLY(D_C0B);
      }
      if (LY == 1) STAMP(48);
      v2u ml = {0u, 0u};
      if (LY == 1) ml = ldx2(rws, o_mg, SM);
      v4u a0 = ldx4(rws, o_cg, SC), a1 = ldx4(rws, o_cg + 1024, SC);
      if (LY == 0) {
        SB();
#pragma unroll
        for (int ks = 0; ks < 2; ++ks)
#pragma unroll
          for (int ct = 0; ct < 4; ++ct) {
            LAS unsigned char* a = L + S_WX + (64 * (w >> 1) + 32 * ks + 8 * u + (n >> 2)) * 128 + ct * 32 + (n & 3) * 8;
            const v2u h0 = __builtin_bit_cast(v2u, __builtin_amdgcn_ds_read_tr16_b64_v4i16((LAS s4v*)a));
            const v2u h1 = __builtin_bit_cast(v2u, __builtin_amdgcn_ds_read_tr16_b64_v4i16((LAS s4v*)(a + 512)));
            wx[4 * ks + ct] = v4u{h0.x, h0.y, h1.x, h1.y};
          }
      }
      const float xres = (g == 0 && hh == 0) ? bflo(*(LAS u16*)(L + S_XB + (64 * cb + l) * 2)) : 0.f;
      bool bad = max(max(vmax(a0), vmax(a1)), max(ml.x, ml.y)) == SENT;
      if (LY == 0) REPOLL(34, bad, (a0 = ldx4(rws, o_cg, SC), a1 = ldx4(rws, o_cg + 1024, SC)), max(vmax(a0), vmax(a1)) == SENT);
      else REPOLL(42, bad, (ml = ldx2(rws, o_mg, SM), a0 = ldx4(rws, o_cg, SC), a1 = ldx4(rws, o_cg + 1024, SC)), max(max(vmax(a0), vmax(a1)), max(ml.x, ml.y)) == SENT);
      STAMP(5 + SO);
      f4 d[4];
#pragma unroll
      for (int ct = 0; ct < 4; ++ct) d[ct] = mfma(a0, wx[ct], f4{0.f, 0.f, 0.f, 0.f});
#pragma unroll
      for (int ct = 0; ct < 4; ++ct) d[ct] = mfma(a1, wx[4 + ct], d[ct]);
      LAS unsigned char* E = L + S_E + (LY == 0 ? 0 : w * 128);
      float e1 = 0.f;
      if (LY == 1) e1 = split_weights(ml, E, l);
      const f4 ev = *(LAS f4*)(E + (16 * (w & 1) + 4 * u) * 4);
      if (LY == 1 && LO) Ls = split_norm(e1, ml);
      float cs[4];
#pragma unroll
      for (int ct = 0; ct < 4; ++ct) cs[ct] = ev[0] * d[ct][0] + ev[1] * d[ct][1] + ev[2] * d[ct][2] + ev[3] * d[ct][3];
      const float z = sw16_add(sw32_add(cs[0], cs[2]), sw32_add(cs[1], cs[3]));
      *(LAS float*)(L + S_PART + w * 256 + l * 4) = z;
      BAR();
      if (LO && w == 0) {
        float s = 0.f;
#pragma unroll
        for (int i = 0; i < 8; ++i) s += *(LAS float*)(L + S_PART + i * 256 + l * 4);
        s = fmaf(s, __builtin_amdgcn_rcpf(Ls), xres);
        const u32 pk = cvtpk(s, dpp<QX1>(s));
#pragma unroll
        for (int r = 0; r < 8; ++r) { if (LY == 0) stx1(pk, rws, o_pub0, r * WD0_REP); else stx1(pk, rws, o_pub1, srep[r]); }
      }
      STAMP(6 + SO);
      if (LY == 1) return;
      if (LO) vote();
      if (LO && w == 1) {
        DLY(D_DSH);
        const u32 o = l < 16 ? W_D0 + g * WD0_REP + l * 896 + 6 * 128 : OOB;
        u32 v = ldx1(rws, o, 0);
        bool badv = v == SENT;
        REPOLL(39, badv, v = ldx1(rws, o, 0), v == SENT);
        if (l == 0) *(LAS u64*)(L + S_TML) = __builtin_amdgcn_s_memrealtime();
      }
      BAR();
      if (!LO) set_sides();
      wait_until(*(LAS u64*)(L + S_TML) + DLV(D_A1));
    } else {
      u64 t_ml = 0;
      if (LY == 0 && LO) vote();
      if (LO && w == 0) {
        v2u ml = ldx2(rws, o_mg, SM);
        bool bad = max(ml.x, ml.y) == SENT;
        REPOLL(34 + SO / 2, bad, ml = ldx2(rws, o_mg, SM), max(ml.x, ml.y) == SENT);
        t_ml = __builtin_amdgcn_s_memrealtime();
        if (l == 0) *(LAS u64*)(L + S_TML) = t_ml;
      }
      if (LY == 1) { BAR(); t_ml = *(LAS u64*)(L + S_TML); }
      STAMP(5 + SO);
      if (LY == 0) {
        if (LO) asm volatile("s_waitcnt vmcnt(0)" ::: "memory");
        if (ROLE == 2) {
          const rsrc_t rw2 = mkrs(p.w2, DM * FF * 2);
          const u32 o = (w < 7 && 128 * wa + 2 * l < DM) ? (224 * g + 32 * w) * (DM * 2) + wa * 256 + l * 4 : OOB;
#pragma unroll
          for (int i = 0; i < 32; ++i) w2_raw[i] = __builtin_amdgcn_raw_buffer_load_b32(rw2, o + i * (DM * 2), 0, NTW);
          SB();
          BAR();
        } else {
          if (!LO) {
            const int tt = t - 256;
            load_wx(WFE, 7);
            SB();
#pragma unroll
            for (int i = 0; i < 7; ++i) { const int idx = tt + 256 * i; *(LAS v4u*)(L + S_WX + ((idx ^ ((idx >> 5) & 7)) * 16)) = wx_raw[i]; }
          }
          BAR();
          v2u d[8];
#pragma unroll
          for (int jj = 0; jj < 8; ++jj) d[jj] = *(LAS v2u*)(L + S_WX + (((32 * lc + 4 * jj + (w >> 1)) ^ (lc & 7)) * 16) + (w & 1) * 8);
#pragma unroll
          for (int q = 0; q < 2; ++q)
#pragma unroll
            for (int i = 0; i < 4; ++i) { wx[2 * q][i] = __builtin_amdgcn_perm(d[2 * i + 1][q], d[2 * i][q], 0x05040100u); wx[2 * q + 1][i] = __builtin_amdgcn_perm(d[2 * i + 1][q], d[2 * i][q], 0x07060302u); }
          S13 = rows4(dot8(wx[0], onem4), dot8(wx[1], onem4), dot8(wx[2], onem4), dot8(wx[3], onem4));
        }
        t_ml = *(LAS u64*)(L + S_TML);
        if (!LO) set_sides();
      }
      wait_until(t_ml + DLV(LY == 0 ? D_DS0 : D_DS1));
      if (LY == 1) STAMP(49);
      v4u dv[4];
      auto dso = [&](int i) { return LY == 0 ? (u32)(i * 4 * 896) : sg + (i & 1) * 4096 + (i >> 1) * 16384; };
      if (LO) {
#pragma unroll
        for (int i = 0; i < 4; ++i) dv[i] = ldx4(rws, o_dg, dso(i));
      }
      if (LY == 0 && ROLE == 2) {
#pragma unroll
        for (int i = 0; i < 16; ++i) { wx[i >> 2][i & 3] = __builtin_amdgcn_perm(w2_raw[2 * i + 1], w2_raw[2 * i], 0x05040100u);
                                       wx[4 + (i >> 2)][i & 3] = __builtin_amdgcn_perm(w2_raw[2 * i + 1], w2_raw[2 * i], 0x07060302u); }
      }
      if (LO) {
        auto dbad = [&]() { return max(max(vmax(dv[0]), vmax(dv[1])), max(vmax(dv[2]), vmax(dv[3]))) == SENT; };
        bool bad = dbad();
        REPOLL(35 + SO / 2, bad, (dv[0] = ldx4(rws, o_dg, dso(0)), dv[1] = ldx4(rws, o_dg, dso(1)), dv[2] = ldx4(rws, o_dg, dso(2)), dv[3] = ldx4(rws, o_dg, dso(3))), dbad());
        STAMP(7 + SO);
        f4 x1 = {0.f, 0.f, 0.f, 0.f};
#pragma unroll
        for (int i = 0; i < 4; ++i) x1 = mfma(selm, dv[i], x1);
        if (a_xw < DM) {
          if (ROLE == 1) *(LAS v2u*)(L + S_XB + a_xw * 2) = v2u{cvtpk(x1[0], x1[1]), cvtpk(x1[2], x1[3])};
          else *(LAS f4*)(L + S_X1 + a_xw * 4) = x1;
        }
      }
      if constexpr (ROLE == 1) {
        BAR();
        const v4u xb = *(LAS v4u*)(L + S_XB + l * 16);
        float mu, r; ln_stats(xb, mu, r);
        float y = (rows4(dot8(xb, wx[0]), dot8(xb, wx[1]), dot8(xb, wx[2]), dot8(xb, wx[3])) - mu * S13) * r;
        if (!isup) y *= __builtin_amdgcn_rcpf(1.f + __builtin_amdgcn_exp2f(-1.4426950408889634f * y));
        auto s = __builtin_amdgcn_permlane16_swap(__float_as_uint(y), __float_as_uint(y), false, false);
        const u32 pk = cvtpk(__uint_as_float(s[0]), __uint_as_float(s[1]));
        auto s2 = __builtin_amdgcn_permlane32_swap(pk, pk, false, false);
        stx2(v2u{s2[0], s2[1]}, rws, o_pub0, SH + sg);
        STAMP(8 + SO);
        if (LY == 1) return;
        DLY(D_A2);
      } else {
        BAR();
        u32 sfin0 = 0, sfin1 = 0;
        if (LY == 1) {
#pragma unroll
          for (int r = 0; r < 4; ++r) if (r == wa) { sfin0 = srep[2 * r]; sfin1 = srep[2 * r + 1]; }
        }
        DLY(LY == 0 ? D_H0 : D_H1);
        u32 hg = ldx1(rws, o_gat, SH + sg), hu = ldx1(rws, o_gat, SH + sg + LH_UP);
        bool bad2 = max(hg, hu) == SENT;
        REPOLL(36 + SO / 2, bad2, (hg = ldx1(rws, o_gat, SH + sg), hu = ldx1(rws, o_gat, SH + sg + LH_UP)), max(hg, hu) == SENT);
        STAMP(8 + SO);
        const u32 hp = cvtpk(bflo(hg) * bflo(hu), bfhi(hg) * bfhi(hu));
        f2 acc = {0.f, 0.f};
        static_for<16>([&](auto I) { constexpr int i = decltype(I)::value;
          const u32 hb = (u32)__builtin_amdgcn_mov_dpp((int)hp, 0x150 + i, 0xf, 0xf, true);
          acc.x = dot2(hb, wx[i >> 2][i & 3], acc.x); acc.y = dot2(hb, wx[4 + (i >> 2)][i & 3], acc.y); });
        *(LAS f2*)(L + S_PART + w * 512 + l * 8) = acc;
        BAR();
        if (LO && w == 0) {
          f2 s = {0.f, 0.f};
          if (g == 0) s = *(LAS f2*)(L + S_X1 + min(128 * wa + 2 * l, DM - 2) * 4);
#pragma unroll
          for (int i = 0; i < 7; ++i) s += *(LAS f2*)(L + S_PART + i * 512 + l * 8);
          const u32 pk = cvtpk(s.x, s.y);
          if (LY == 0) {
#pragma unroll
            for (int r = 0; r < 8; ++r) stx1(pk, rws, o_pub0, srep[r]);
          } else {
            stx1(pk, rws, o_pub0, SA + sfin0); stx1(pk, rws, o_pub0, SA + sfin1);
          }
        }
        STAMP(9 + SO);
        if (LY == 1) {
          if (LO && w == 0 && j == 28 && g < 7) {
            DLY(D_F);
            const u32 o = n < 8 ? two(L_A + g * LA_REP + u * 1024 + g * 128 + n * 16) : OOB;
            v4u f0 = ldx4(rws, o, SA + sg), f1 = ldx4(rws, o, SA + sg + 4096);
            bool bad3 = max(vmax(f0), vmax(f1)) == SENT;
            REPOLL(45, bad3, (f0 = ldx4(rws, o, SA + sg), f1 = ldx4(rws, o, SA + sg + 4096)), max(vmax(f0), vmax(f1)) == SENT);
            const f4 y = mfma(selm, f1, mfma(selm, f0, f4{0.f, 0.f, 0.f, 0.f}));
            if (u < 2 && n < 8) *(v2u*)(p.out + 64 * g + 8 * n + 4 * u) = v2u{cvtpk(y[0], y[1]), cvtpk(y[2], y[3])};
            if (g == 0 && l == 0) stx1(T0, rws, WS_EP, 0);
            STAMP(10 + SO);
          }
          return;
        }
        DLY(D_A3);
      }
    }
    {
      if (LO) {
        v4u a0 = ldx4(rws, o_ag, sg), a1 = ldx4(rws, o_ag, sg + 4096);
        bool bad = max(vmax(a0), vmax(a1)) == SENT;
        REPOLL(37, bad, (a0 = ldx4(rws, o_ag, sg), a1 = ldx4(rws, o_ag, sg + 4096)), max(vmax(a0), vmax(a1)) == SENT);
        STAMP(10 + SO);
        const f4 x2 = mfma(selm, a1, mfma(selm, a0, f4{0.f, 0.f, 0.f, 0.f}));
        if (a_xw < DM) *(LAS v2u*)(L + S_XB + a_xw * 2) = v2u{cvtpk(x2[0], x2[1]), cvtpk(x2[2], x2[3])};
      }
      BAR();
      if (LO) {
        v4u xb = *(LAS v4u*)(L + S_XB + l * 16);
        const v2u qv = q_phase(xb);
        stx2(qv, rws, o_qs1, sg);
        STAMP(17);
        DLY(D_B1);
      }
    }
  };
  layer(std::integral_constant<int, 0>{});
  layer(std::integral_constant<int, 1>{});
  STAMP_FLUSH();
  asm volatile("; end of program %0 %1" :: "n"(ROLE), "n"(LO ? 1 : 0));
}

__global__ void __launch_bounds__(NT, 2) mega(Params p) {
  extern __shared__ __attribute__((aligned(16))) unsigned char lds_[];
  LAS unsigned char* L = (LAS unsigned char*)lds_;
  const int c = blockIdx.x, t = threadIdx.x, w = __builtin_amdgcn_readfirstlane(t >> 6), j = c >> 3;
  u64 code; u32 len;
  asm volatile("s_getpc_b64 %0\n.Lmk_pc:\n\ts_mov_b32 %1, " FUNC_END "-.Lmk_pc" : "=s"(code), "=s"(len));
  if (w >= 4) {
    Pre pre; prologue<false>(p, L, c, t, w, code, len, pre);
    if (j < 14) program<0, false>(p, L, c, t, w, pre); else if (j < 28) program<1, false>(p, L, c, t, w, pre); else program<2, false>(p, L, c, t, w, pre);
  } else {
    Pre pre; prologue<true>(p, L, c, t, w, code, len, pre);
    if (j < 14) program<0, true>(p, L, c, t, w, pre); else if (j < 28) program<1, true>(p, L, c, t, w, pre); else program<2, true>(p, L, c, t, w, pre);
  }
}
__device__ __attribute__((noinline, used)) void code_end() { asm volatile("mk_code_end:"); }
}

extern "C" void kernel_launch(void* const* d_in, const int* in_sizes, int n_in, void* d_out, int out_size, void* d_ws, size_t ws_size, hipStream_t stream) {
  mk::Params p;
  p.x = (const mk::u16*)d_in[0]; p.k = (const mk::u16*)d_in[1]; p.v = (const mk::u16*)d_in[2];
  p.wq = (const mk::u16*)d_in[3]; p.wo = (const mk::u16*)d_in[4];
  p.w1 = (const mk::u16*)d_in[6]; p.w3 = (const mk::u16*)d_in[7]; p.w2 = (const mk::u16*)d_in[8];
  p.out = (mk::u16*)d_out; p.ws = (unsigned char*)d_ws;
  (void)hipFuncSetAttribute((const void*)mk::mega, hipFuncAttributeMaxDynamicSharedMemorySize, mk::LDS_BYTES);
  hipLaunchKernelGGL(mk::mega, dim3(256), dim3(mk::NT), mk::LDS_BYTES, stream, p);
}
```

```cpp
#include <hip/hip_runtime.h>
#include <stdint.h>
#include <type_traits>

namespace mk {
typedef unsigned u32; typedef unsigned short u16; typedef unsigned long long u64;
typedef u32 v4u __attribute__((ext_vector_type(4)));
typedef u32 v2u __attribute__((ext_vector_type(2)));
typedef float f2 __attribute__((ext_vector_type(2)));
typedef float f4 __attribute__((ext_vector_type(4)));
typedef short s4v __attribute__((ext_vector_type(4)));
typedef __bf16 bf2 __attribute__((ext_vector_type(2)));
typedef __bf16 bf8 __attribute__((ext_vector_type(8)));
#define LAS __attribute__((address_space(3)))
#define DEV __device__ __forceinline__

constexpr int DM = 448, SK = 8192, DH = 256, WIN = 2048, FF = 1792, NQ = 4096;
constexpr int NT = 512;
constexpr int WS_EP = 0;
constexpr int WS_RDY = 4096;
constexpr int W_Q0 = 8192, WQ0_GROUP = 8192;
constexpr int W_C0 = W_Q0 + 8 * WQ0_GROUP;
constexpr int W_ML0 = W_C0 + 262144;
constexpr int W_D0 = W_ML0 + 65536, WD0_REP = 16384;
constexpr int W_TWO = (W_D0 + 8 * WD0_REP + 16383) / 16384 * 16384;
__host__ __device__ constexpr int two(int o) { return W_TWO + ((o >> 13) << 14) + (o & 8191); }
constexpr int L_Q1 = 0, LQ1_GROUP = 4096;
constexpr int L_C1 = L_Q1 + 8 * LQ1_GROUP;
constexpr int L_ML1 = L_C1 + 262144;
constexpr int L_D1 = L_ML1 + 8192, LD1_REP = 16384;
constexpr int L_H = L_D1 + 8 * LD1_REP, LH_LAYER = 8192, LH_UP = 4096;
constexpr int L_A = L_H + 2 * LH_LAYER, LA_REP = 8192, LA_LAYER = 8 * LA_REP;
constexpr int L_PR = L_A + 2 * LA_LAYER;
constexpr int WS_BYTES = W_TWO + 2 * (L_PR + 131072);
static_assert(WS_BYTES <= (2 << 20), "one 2-MB page");
constexpr int WS_STAMP = 4 << 20;
constexpr u32 SENT = 0xFFFFFFFFu;
constexpr u32 OOB = 0x80000000u;

constexpr int S_WQ = 0;
constexpr int S_K = S_WQ + 16384;
constexpr int S_V = S_K + 32768;
constexpr int S_WX = S_V + 32768;
constexpr int S_Q = S_WX + 32768;
constexpr int S_SC = S_Q + 1280;
constexpr int S_P = S_SC + 640;
constexpr int S_E = S_P + 8 * 320;
constexpr int S_PART = S_E + 8 * 128;
constexpr int S_XB = S_PART + 4096;
constexpr int S_X1 = S_XB + 1024;
constexpr int S_HB = S_X1 + 1792;
constexpr int S_CNT = S_HB + 32;
constexpr int S_EP = S_HB + 48;
constexpr int S_TML = S_HB + 64;
constexpr int S_END = S_HB + 80;
constexpr int LDS_BYTES = (S_END + 255) / 256 * 256;
static_assert(LDS_BYTES > 81920 && LDS_BYTES <= 163840, "LDS must pin one workgroup per CU");

struct Params {
  const u16 *x, *k, *v, *wq, *wo, *w1, *w3, *w2;
  u16* out;
  unsigned char* ws;
};

#define SPIN_CAP (1u << 18)
#define BACKOFF 1
#define NTW 2
#define WARM 1
#define WARM_AUX 2
enum { D_B0, D_TB0, D_B1, D_C0, D_C1, D_DS0, D_DS1, D_H0, D_H1, D_A1, D_A2, D_A3, D_F, D_C0B, D_DSH, D_N };
#define DL_DEFAULT {20, 340, 18, 24, 18, 60, 62, 54, 48, 110, 60, 12, 8, 2, 30}
#define PROBE_THR 110
#define FUNC_END "mk_code_end"
#define VSLEEP 0
#define CODEPF 1
#define WOE 8
#define WFE 4
#define DLV(k) dl_default[k]
constexpr int dl_default[D_N] = DL_DEFAULT;

DEV float bflo(u32 v) { return __uint_as_float(v << 16); }
DEV u32 cvtpk(float a, float b) { f2 v = {a, b}; bf2 r = __builtin_convertvector(v, bf2); return __builtin_bit_cast(u32, r); }
DEV float dot2(u32 a, u32 b, float c) { return __builtin_amdgcn_fdot2_f32_bf16(__builtin_bit_cast(bf2, a), __builtin_bit_cast(bf2, b), c, false); }
DEV f4 mfma(v4u a, v4u b, f4 c) { return __builtin_amdgcn_mfma_f32_16x16x32_bf16(__builtin_bit_cast(bf8, a), __builtin_bit_cast(bf8, b), c, 0, 0, 0); }
template <int C> DEV float dpp(float x) { return __builtin_bit_cast(float, __builtin_amdgcn_mov_dpp(__builtin_bit_cast(int, x), C, 0xf, 0xf, true)); }
constexpr int ROR8 = 0x128, ROR4 = 0x124, ROR2 = 0x122, ROR1 = 0x121, QX1 = 0xB1, QX2 = 0x4E, HMIR = 0x141;
DEV float sw32_add(float a, float b) { auto s = __builtin_amdgcn_permlane32_swap(__float_as_uint(a), __float_as_uint(b), false, false); return __uint_as_float(s[0]) + __uint_as_float(s[1]); }
DEV float sw16_add(float a, float b) { auto s = __builtin_amdgcn_permlane16_swap(__float_as_uint(a), __float_as_uint(b), false, false); return __uint_as_float(s[0]) + __uint_as_float(s[1]); }
DEV float vmaxf(float a, float b) { return __builtin_amdgcn_fmed3f(a, b, __builtin_inff()); }
DEV float sw32_max(float a, float b) { auto s = __builtin_amdgcn_permlane32_swap(__float_as_uint(a), __float_as_uint(b), false, false); return vmaxf(__uint_as_float(s[0]), __uint_as_float(s[1])); }
DEV float sw16_max(float a, float b) { auto s = __builtin_amdgcn_permlane16_swap(__float_as_uint(a), __float_as_uint(b), false, false); return vmaxf(__uint_as_float(s[0]), __uint_as_float(s[1])); }
DEV float row_sum(float z) { z += dpp<ROR8>(z); z += dpp<ROR4>(z); z += dpp<ROR2>(z); z += dpp<ROR1>(z); return z; }
DEV float row_max(float z) { z = vmaxf(z, dpp<ROR8>(z)); z = vmaxf(z, dpp<ROR4>(z)); z = vmaxf(z, dpp<ROR2>(z)); z = vmaxf(z, dpp<ROR1>(z)); return z; }
DEV float sum4(float a, float b, float c, float d) { return row_sum(sw16_add(sw32_add(a, c), sw32_add(b, d))); }
DEV float rdl(float v, int l) { return __uint_as_float(__builtin_amdgcn_readlane(__float_as_uint(v), l)); }
DEV void pin(u32& v) { asm volatile("" : "+v"(v)); }
DEV void pin(v4u& v) { asm volatile("" : "+v"(v)); }
#define SB() __builtin_amdgcn_sched_barrier(0)
#define BAR() do { asm volatile("s_waitcnt lgkmcnt(0)" ::: "memory"); __builtin_amdgcn_s_barrier(); asm volatile("" ::: "memory"); } while (0)
DEV void wait_until(u64 t) { asm volatile("" ::: "memory"); while (__builtin_amdgcn_s_memrealtime() < t) {} asm volatile("" ::: "memory"); SB(); }
#define DLY(k) wait_until(__builtin_amdgcn_s_memrealtime() + DLV(k))
#define CNT(i)
#define STAMP(i)
#define STAMP_FLUSH()
#define REPOLL(id, bad, LOADS, BAD) do { if (__builtin_expect(__any(bad), 0)) { for (u32 sp_ = 0; sp_ < SPIN_CAP; ++sp_) { asm volatile("" ::: "memory"); CNT(id); \
    if (BACKOFF) __builtin_amdgcn_s_sleep(BACKOFF); LOADS; if (!__any(BAD)) break; } } asm volatile("" ::: "memory"); } while (0)

typedef __amdgpu_buffer_rsrc_t rsrc_t;
DEV rsrc_t mkrs(const void* p, u32 bytes) { return __builtin_amdgcn_make_buffer_rsrc((void*)p, 0, bytes, 0x00020000); }
DEV v4u ldw(rsrc_t r, u32 off) { return __builtin_amdgcn_raw_buffer_load_b128(r, off, 0, NTW); }
DEV v4u ldx4(rsrc_t r, u32 off, u32 so) { return __builtin_amdgcn_raw_buffer_load_b128(r, off, so, 16); }
DEV v2u ldx2(rsrc_t r, u32 off, u32 so) { return __builtin_amdgcn_raw_buffer_load_b64(r, off, so, 16); }
DEV u32 ldx1(rsrc_t r, u32 off, u32 so) { return __builtin_amdgcn_raw_buffer_load_b32(r, off, so, 16); }
DEV void stx4(v4u v, rsrc_t r, u32 off, u32 so) { __builtin_amdgcn_raw_buffer_store_b128(v, r, off, so, 16); }
DEV void stx2(v2u v, rsrc_t r, u32 off, u32 so) { __builtin_amdgcn_raw_buffer_store_b64(v, r, off, so, 16); }
DEV void stx1(u32 v, rsrc_t r, u32 off, u32 so) { __builtin_amdgcn_raw_buffer_store_b32(v, r, off, so, 16); }
DEV u32 vmax(v4u v) { return max(max(max(v.x, v.y), v.z), v.w); }
constexpr u32 ONE = 0x3F803F80u;

DEV void read_cols(LAS unsigned char* base, int lc, int w, v4u& wa, v4u& wb) {
  u32 d[8];
#pragma unroll
  for (int jj = 0; jj < 8; ++jj) d[jj] = *(LAS u32*)(base + (((16 * lc + 2 * jj + (w >> 2)) ^ (lc & 7)) * 16) + (w & 3) * 4);
#pragma unroll
  for (int i = 0; i < 4; ++i) { wa[i] = __builtin_amdgcn_perm(d[2 * i + 1], d[2 * i], 0x05040100u); wb[i] = __builtin_amdgcn_perm(d[2 * i + 1], d[2 * i], 0x07060302u); }
}
DEV float dot8(const v4u x, const v4u wv) { float a = 0.f;
#pragma unroll
  for (int i = 0; i < 4; ++i) a = dot2(x[i], wv[i], a);
  return a; }
DEV float rows4(float a, float b, float c, float d) { return row_sum(sw16_add(sw32_add(a, c), sw32_add(b, d))); }
DEV void ln_stats(const v4u x, float& mu, float& r) {
  float st = sw32_add(dot8(x, v4u{ONE, ONE, ONE, ONE}), dot8(x, x)); st = row_sum(sw16_add(st, st));
  mu = rdl(st, 0) * (1.f / DM);
  r = __builtin_amdgcn_rsqf(fmaxf(fmaf(rdl(st, 32), 1.f / DM, -mu * mu), 0.f) + 1e-5f);
}
DEV float reduce_rows(const float (&v8)[8], int l) {
  float v4_[4], v2_[2];
#pragma unroll
  for (int i = 0; i < 4; ++i) v4_[i] = sw32_add(v8[i], v8[i + 4]);
#pragma unroll
  for (int i = 0; i < 2; ++i) v2_[i] = sw16_add(v4_[i], v4_[i + 2]);
  const bool k8 = l & 8;
  return (k8 ? v2_[1] : v2_[0]) + dpp<ROR8>(k8 ? v2_[0] : v2_[1]);
}
template <int N, int I = 0, class F> DEV void static_for(F&& f) { if constexpr (I < N) { f(std::integral_constant<int, I>{}); static_for<N, I + 1>(f); } }
DEV float split_weights(const v2u ml, LAS unsigned char* E, int l) {
  const float mv = __uint_as_float(ml.x);
  float M = row_max(mv); M = sw16_max(M, M);
  const float e = __builtin_amdgcn_exp2f(mv - M);
  *(LAS float*)(E + (l & 31) * 4) = e;
  return e;
}
DEV float split_norm(float e, const v2u ml) { float Ls = row_sum(e * __uint_as_float(ml.y)); return sw16_add(Ls, Ls); }
DEV v4u sel_matrix(int n) { v4u a = {0u, 0u, 0u, 0u};
#pragma unroll
  for (int i = 0; i < 4; ++i) a[i] = (n >> 1) == i ? ((n & 1) ? 0x3F800000u : 0x00003F80u) : 0u;
  return a; }
DEV float bfhi(u32 v) { return __uint_as_float(v & 0xFFFF0000u); }

struct Pre {
  v4u xp, ep, wq_raw[4];
  v4u kv_raw[8];
  u64 t_start;
  u32 code_pf;
};
template <bool LO>
DEV void prologue1(const Params& p, LAS unsigned char* L, const int c, const int t, const int w, const u64 code, const u32 code_len, Pre& s) {
  const int g = c & 7, j = c >> 3, l = t & 63;
  if constexpr (LO) {
    const rsrc_t rws = mkrs(p.ws, WS_BYTES);
    const rsrc_t rwq = mkrs(p.wq, DM * NQ * 2);
    const u32 o = (112 * (j >> 3) + (t >> 3)) * (NQ * 2) + (g * 512 + (j & 7) * 64) * 2 + (t & 7) * 16;
#pragma unroll
    for (int i = 0; i < 4; ++i) s.wq_raw[i] = ldw(rwq, (i < 3 || t < 128) ? o + i * 32 * (NQ * 2) : OOB);
    s.xp = __builtin_amdgcn_raw_buffer_load_b128(mkrs(p.x, DM * 2), w == 0 ? l * 16 : OOB, 0, 0);
    s.ep = ldx4(rws, (w == 0 && l == 0) ? WS_EP : OOB, 0);
    SB();
    const int hh = j >= 7, wa = j - 28;
    u32 oc = OOB;
    if (w == 0) oc = W_C0 + g * 32768 + j * 1024 + l * 16;
    if (w == 1 && j < 14) oc = W_D0 + (l >> 3) * WD0_REP + (2 * g + hh) * 896 + (j - 7 * hh) * 128 + (l & 7) * 16;
    if (w == 2 && l < 8) oc = two(L_PR + c * 512 + (l >> 1) * 128) + (l & 1) * 8192;
    if (w == 3 && l < 2) oc = W_ML0 + ((2 * g + l) * 32 + j) * 128;
    stx4(v4u{SENT, SENT, SENT, SENT}, rws, oc, 0);
    oc = OOB;
    if (j >= 14 && j < 28 && w == 0 && l < 8) oc = two(L_H + (j >= 21 ? LH_UP : 0) + (7 * g + (j - (j >= 21 ? 21 : 14))) * 64 + (l & 3) * 16) + (l >> 2) * 8192;
    if (j >= 28 && (l & 15) + 16 * wa < 56) oc = two(L_A + (2 * w + (l >> 5)) * LA_REP + g * 1024 + wa * 256 + (l & 15) * 16) + ((l >> 4) & 1) * 8192;
    stx4(v4u{SENT, SENT, SENT, SENT}, rws, oc, 0);
    if (t == 0) *(LAS v4u*)(L + S_CNT) = v4u{0u, 0u, 0u, 0u};
    asm volatile("s_waitcnt lgkmcnt(0)\n\t; first stage, waves 0..3" ::: "memory");
  } else {
    s.code_pf = 0;
    if (CODEPF) s.code_pf = __builtin_amdgcn_raw_buffer_load_b32(mkrs((const void*)code, code_len & ~3u), w == 7 ? (j + 32 * l) * 128 : OOB, 0, 0);
    asm volatile("; first stage, waves 4..7" ::: "memory");
  }
}
DEV void prologue2(const Params& p, const int c, const int t, const bool lo, Pre& s) {
  const int g = c & 7, j = c >> 3;
  const u32 kvbase = ((u32)g * SK + (SK - WIN) + j * 64) * (DH * 2);
  const rsrc_t r = mkrs(lo ? p.k : p.v, 8u * SK * DH * 2);
#pragma unroll
  for (int i = 0; i < 8; ++i) s.kv_raw[i] = ldw(r, kvbase + ((t & 255) + 256 * i) * 16);
}

template <int ROLE, bool LO>
DEV void program(const Params& p, LAS unsigned char* L, const int c, const int t, const int w, Pre& pre) {
  const int g = c & 7, j = c >> 3, l = t & 63, u = l >> 4, n = l & 15;
  asm volatile("; start of program %0 %1" :: "n"(ROLE), "n"(LO ? 1 : 0) : "memory");
  const u64 t_start = pre.t_start;
  const rsrc_t rws = mkrs(p.ws, WS_BYTES);
  const int hh = j >= 7, h = 2 * g + hh, cb = j - 7 * hh;
  const bool isup = j >= 21;
  const int ub = 7 * g + (j - (isup ? 21 : 14));
  const int wa = j - 28;
  const int kq = j >> 3, cq = j & 7;
  v4u xp = pre.xp; const v4u ep = pre.ep;
  v4u (&wq_raw)[4] = pre.wq_raw; v4u (&k_raw)[8] = pre.kv_raw; v4u (&v_raw)[8] = pre.kv_raw;
  const u32 code_pf = pre.code_pf;
  v4u wx_raw[8];
  STAMP(13);
  u32 o_qs0 = n == 0 ? W_Q0 + g * WQ0_GROUP + kq * 2048 + (16 * cq + 4 * w + u) * 16 : OOB;
  u32 o_qs1 = n == 0 ? two(L_Q1 + g * LQ1_GROUP + kq * 1024 + (64 * cq + 16 * w + 4 * u) * 2) : OOB;
  u32 o_qg0 = W_Q0 + g * WQ0_GROUP + (l >> 5) * 4096 + (32 * w + (l & 31)) * 16;
  u32 o_qg1 = two(L_Q1 + g * LQ1_GROUP + u * 1024 + (128 * w + 8 * n) * 2);
  const int cofs = (2 * g + n) * 16384 + (j >> 4) * 8192 + w * 1024 + (u * 16 + (j & 15)) * 16;
  u32 o_cs0 = n < 2 ? W_C0 + cofs : OOB, o_cs1 = n < 2 ? two(L_C1 + cofs) : OOB;
  u32 o_ms0 = (l & 31) == 0 ? W_ML0 + ((2 * g + (l >> 5)) * 32 + j) * 128 : OOB, o_ms1 = (l & 31) == 0 ? two(L_ML1 + ((2 * g + (l >> 5)) * 32 + j) * 8) : OOB;
  const int hx = ROLE == 0 ? h : 2 * g;
  const int gofs = hx * 16384 + (w & 1) * 8192 + (w >> 1) * 2048 + l * 16;
  u32 o_cg0 = W_C0 + gofs, o_cg1 = two(L_C1 + gofs);
  u32 o_mg0 = W_ML0 + (hx * 32 + (l & 31)) * 128, o_mg1 = two(L_ML1 + (hx * 32 + (l & 31)) * 8);
  u32 o_pub0 = OOB, o_pub1 = OOB, o_gat = OOB;
  if (ROLE == 0) { o_pub0 = (l & 1) == 0 ? W_D0 + h * 896 + cb * 128 + (l >> 1) * 4 : OOB;
                   o_pub1 = (l & 1) == 0 ? two(L_D1 + h * 1024 + cb * 128 + (l >> 1) * 4) : OOB; }
  else if (ROLE == 1) o_pub0 = l == 0 ? two(L_H + (isup ? LH_UP : 0) + ub * 64 + w * 8) : OOB;
  else { o_pub0 = 128 * wa + 2 * l < DM ? two(L_A + g * 1024 + wa * 256 + l * 4) : OOB;
         o_gat = w < 7 ? two(L_H + (224 * g + 32 * w) * 2 + n * 4) : OOB; }
  const bool cin = LO && 128 * w + 8 * n < DM;
  u32 o_dg0 = cin ? W_D0 + g * WD0_REP + u * 896 + w * 256 + n * 16 : OOB;
  u32 o_dg1 = cin ? two(L_D1 + g * LD1_REP + u * 1024 + w * 256 + n * 16) : OOB;
  u32 o_ag = cin ? two(L_A + g * LA_REP + u * 1024 + w * 256 + n * 16) : OOB;
  u32 a_xw = (cin && u < 2) ? (128 * w + 8 * n + 4 * u) : 0xFFFF;
  v4u selm = sel_matrix(n);
  pin(o_cs0); pin(o_cs1); pin(o_mg0); pin(o_mg1); pin(o_pub0);
  if (LO) { pin(o_ag); pin(a_xw); pin(selm); pin(o_qs0); pin(o_qs1); pin(o_qg0); pin(o_qg1); pin(o_ms0); pin(o_ms1); }
  if (ROLE == 0) { pin(o_cg0); pin(o_cg1); pin(o_pub1); }
  if (ROLE == 2) pin(o_gat);
  if (ROLE != 0 && LO) { pin(o_dg0); pin(o_dg1); }
  const int lc = min(l, 55);
  const u32 onem = l < 56 ? ONE : 0u;
  const v4u onem4 = {onem, onem, onem, onem};
  SB();
  u32 T0 = 0, T1 = 0, R0 = 0, R1 = 0, R2 = 0, R3 = 0;
  auto lo_sync = [&](u32 target) {
    asm volatile("s_waitcnt lgkmcnt(0)" ::: "memory");
    if (l == 0) __hip_atomic_fetch_add((LAS u32*)(L + S_CNT), 1u, __ATOMIC_RELAXED, __HIP_MEMORY_SCOPE_WORKGROUP);
    while (*(volatile LAS u32*)(L + S_CNT) < target) {}
    asm volatile("" ::: "memory");
  };
  v4u kf[8];
  v4u wqf[4];
  f4 Sq;
  auto q_phase = [&](const v4u xs) {
    f4 acc = {0.f, 0.f, 0.f, 0.f};
#pragma unroll
    for (int s = 0; s < 4; ++s) acc = mfma(wqf[s], *(LAS v4u*)(L + S_XB + (112 * kq + 32 * s + 8 * u) * 2), acc);
    float mu, r; ln_stats(xs, mu, r);
    return v2u{cvtpk((acc[0] - mu * Sq[0]) * r, (acc[1] - mu * Sq[1]) * r), cvtpk((acc[2] - mu * Sq[2]) * r, (acc[3] - mu * Sq[3]) * r)};
  };
  if constexpr (LO) {
#pragma unroll
    for (int i = 0; i < 4; ++i) *(LAS v4u*)(L + S_WQ + (t + 256 * i) * 16) = wq_raw[i];
    if (w == 0) { *(LAS v4u*)(L + S_XB + l * 16) = xp; if (l == 0) *(LAS v4u*)(L + S_EP) = ep; }
    lo_sync(4);
    STAMP(11);
    xp = *(LAS v4u*)(L + S_XB + l * 16);
    { const v4u e = *(LAS v4u*)(L + S_EP);
      T0 = __builtin_amdgcn_readfirstlane(e.x) + 1u; T1 = __builtin_amdgcn_readfirstlane(e.y); const u32 T2 = __builtin_amdgcn_readfirstlane(e.z);
      R0 = T0 << 3; R1 = (T0 * 0x9E3779B1u ^ T1) & ~7u; R2 = (T0 * 0x85EBCA77u ^ T2) & ~7u; R3 = (T0 * 0xC2B2AE3Du ^ T1 ^ (T2 >> 7)) & ~7u; }
#pragma unroll
    for (int s = 0; s < 4; ++s) {
      LAS unsigned char* a = L + S_WQ + (32 * s + 8 * u + (n >> 2)) * 128 + w * 32 + (n & 3) * 8;
      const v2u h0 = __builtin_bit_cast(v2u, __builtin_amdgcn_ds_read_tr16_b64_v4i16((LAS s4v*)a));
      const v2u h1 = __builtin_bit_cast(v2u, __builtin_amdgcn_ds_read_tr16_b64_v4i16((LAS s4v*)(a + 512)));
      wqf[s] = v4u{h0.x, h0.y, h1.x, h1.y};
    }
    Sq = f4{0.f, 0.f, 0.f, 0.f};
#pragma unroll
    for (int s = 0; s < 4; ++s) Sq = mfma(wqf[s], v4u{ONE, ONE, ONE, ONE}, Sq);
    { const v2u qv = q_phase(xp);
      asm volatile("s_waitcnt vmcnt(8)" ::: "memory");
      stx4(v4u{qv.x, qv.y, T0, T1}, rws, o_qs0, 0); }
    STAMP(1);
#pragma unroll
    for (int i = 0; i < 8; ++i) { const int idx = t + 256 * i, key = idx >> 5; *(LAS v4u*)(L + S_K + key * 512 + (((idx & 31) ^ (key & 15)) * 16)) = k_raw[i]; }
    lo_sync(8);
#pragma unroll
    for (int s = 0; s < 8; ++s) kf[s] = *(LAS v4u*)(L + S_K + (16 * w + n) * 512 + (((4 * s + u) ^ n) * 16));
    {
      const u32 first = (j ^ w) & 1, oa = two(L_PR + c * 512 + w * 128) + first * 8192, ob = two(L_PR + c * 512 + w * 128) + (first ^ 1) * 8192;
      u32 va = ldx1(rws, l == 0 ? oa : OOB, 0), vb = ldx1(rws, l == 0 ? ob : OOB, 0);
      SB();
      asm volatile("s_waitcnt vmcnt(1)" : "+v"(va) :: "memory");
      const u64 t1 = __builtin_amdgcn_s_memtime();
      asm volatile("s_waitcnt lgkmcnt(0)\n\ts_waitcnt vmcnt(0)" : "+v"(vb) :: "memory");
      const u64 t2 = __builtin_amdgcn_s_memtime();

      u32 xcc; asm volatile("s_getreg_b32 %0, hwreg(HW_REG_XCC_ID, 0, 4)" : "=s"(xcc));
      const u32 die = (xcc >> 2) & 1;
      const u32 vt = (u32)(t2 - t1) > PROBE_THR ? ((first ^ die) ? 1u : 2u) : 0u;
      __builtin_amdgcn_raw_buffer_store_b32((w == 0 ? R0 : w == 1 ? R1 : w == 2 ? R2 : R3) | (die << 2) | vt, rws, l == 0 ? WS_RDY + c * 16 + w * 4 : OOB, 0, 16);
    }
    STAMP(14);
  }

  u32 sg = 0, srep[8];
  auto set_sides = [&]() {
    const v4u cw = *(LAS v4u*)(L + S_CNT);
    const u32 sm = __builtin_amdgcn_readfirstlane(((int)cw.z > 0 ? ~cw.w : cw.w) & 0xFFu);
    sg = ((sm >> g) & 1u) << 13;
#pragma unroll
    for (int r = 0; r < 8; ++r) srep[r] = r * (ROLE == 0 ? 2 * LD1_REP : 2 * LA_REP) + (((sm >> r) & 1u) << 13);
  };
  auto vote = [&]() {
    v4u rdy = ldx4(rws, WS_RDY + t * 16, 0);
    auto rbad = [&]() { return ((rdy.x & ~7u) != R0) | ((rdy.y & ~7u) != R1) | ((rdy.z & ~7u) != R2) | ((rdy.w & ~7u) != R3); };
    bool bad = rbad();
    REPOLL(33, bad, rdy = ldx4(rws, WS_RDY + t * 16, 0), rbad());
    const int votes = __popcll(__ballot(rdy.x & 1u)) + __popcll(__ballot(rdy.y & 1u)) + __popcll(__ballot(rdy.z & 1u)) + __popcll(__ballot(rdy.w & 1u))
                    - __popcll(__ballot(rdy.x & 2u)) - __popcll(__ballot(rdy.y & 2u)) - __popcll(__ballot(rdy.z & 2u)) - __popcll(__ballot(rdy.w & 2u));
    const u64 dies = __ballot(rdy.w & 4u);
    if (l == 0) { __hip_atomic_fetch_add((LAS u32*)(L + S_CNT + 8), (u32)votes, __ATOMIC_RELAXED, __HIP_MEMORY_SCOPE_WORKGROUP);
                  if (w == 0) *(LAS u32*)(L + S_CNT + 12) = (u32)dies & 0xFFu; }
    lo_sync(16);
    set_sides();
    if (ROLE != 0) {
      const int m = j - 14, ch = 2 * m + w;
      u32 oc = OOB;
      if (w < 2) oc = two(ch < 32 ? L_C1 + g * 32768 + ch * 1024 + l * 16 : L_Q1 + g * LQ1_GROUP + (ch - 32) * 1024 + l * 16);
      else if (w == 2) {
        if (m < 16) oc = two(L_D1 + g * LD1_REP + m * 1024 + l * 16);
        else if (m == 16) { if (l < 32) oc = two(L_ML1 + g * 512 + l * 16); }
        else if ((l & 31) < 28) oc = two(L_H + LH_LAYER + (l >> 5) * LH_UP + g * 448 + (l & 31) * 16);
      } else if (m < 8) oc = two(L_A + LA_LAYER + g * LA_REP + m * 1024 + l * 16);
      stx4(v4u{SENT, SENT, SENT, SENT}, rws, oc == OOB ? OOB : oc + sg, 0);
    }
  };

  v4u vf[2][2];
  float S13 = 0.f;
  v4u wx[8];
  u32 w2_raw[32];

  auto load_wx = [&](int i0, int i1) {
    const int tt = t - 256;
    if (ROLE == 0) {
      const rsrc_t rwo = mkrs(p.wo, DM * NQ * 2);
#pragma unroll
      for (int i = 0; i < 8; ++i) if (i >= i0 && i < i1) wx_raw[i] = ldw(rwo, (h * 256 + (tt >> 3) + 32 * i) * (DM * 2) + cb * 128 + (tt & 7) * 16);
    } else if (ROLE == 1) {
      const rsrc_t r1 = mkrs(isup ? p.w3 : p.w1, DM * FF * 2);
      const u32 o = (tt >> 2) * (FF * 2) + ub * 64 + (tt & 3) * 16;
#pragma unroll
      for (int i = 0; i < 7; ++i) if (i >= i0 && i < i1) wx_raw[i] = ldw(r1, o + i * 64 * (FF * 2));
    }
  };

  auto layer = [&](auto LC) {
    constexpr int LY = decltype(LC)::value;
    constexpr u32 SH = LY * 2 * LH_LAYER, SA = LY * 2 * LA_LAYER;
    constexpr int SO = 16 * LY;
    const u32 o_ms = LY == 0 ? o_ms0 : o_ms1, o_mg = LY == 0 ? o_mg0 : o_mg1, o_cs = LY == 0 ? o_cs0 : o_cs1, o_cg = LY == 0 ? o_cg0 : o_cg1, o_dg = LY == 0 ? o_dg0 : o_dg1;
    const u32 SM = LY == 0 ? 0u : sg, SC = SM;
    if (LO) {
      if (LY == 0) {
        DLY(D_B0);
        wait_until(t_start + DLV(D_TB0));
        auto qbad = [&](const v4u& q) { return (q.z != T0) | (q.w != T1); };
        v4u q0 = ldx4(rws, o_qg0, 0), q1 = ldx4(rws, o_qg0 + 2048, 0);
        SB();
        if (l == 0) __hip_atomic_fetch_add((LAS u32*)(L + S_CNT + 4), 1u, __ATOMIC_RELAXED, __HIP_MEMORY_SCOPE_WORKGROUP);
        SB();
        bool bad = qbad(q0) | qbad(q1);
        REPOLL(32, bad, (q0 = ldx4(rws, o_qg0, 0), q1 = ldx4(rws, o_qg0 + 2048, 0)), qbad(q0) | qbad(q1));
        const float c01 = sw32_add(bflo(q0.x) + bflo(q1.x), bflo(q0.y) + bflo(q1.y));
        const float c13 = sw32_add(bfhi(q0.x) + bfhi(q1.x), bfhi(q0.y) + bfhi(q1.y));
        *(LAS u32*)(L + S_Q + (w >> 1) * 576 + ((32 * w + (l & 31)) & 63) * 8 + (l >> 5) * 4) = cvtpk(c01, c13);
        lo_sync(12);
      } else {
        if (LY == 1) STAMP(50);
        v4u q0 = ldx4(rws, o_qg1, sg);
        bool bad = vmax(q0) == SENT;
        REPOLL(40, bad, q0 = ldx4(rws, o_qg1, sg), vmax(q0) == SENT);
        const f4 s = mfma(selm, q0, f4{0.f, 0.f, 0.f, 0.f});
        if (u < 2) *(LAS v2u*)(L + S_Q + (w >> 1) * 576 + ((128 * w + 8 * n + 4 * u) & 255) * 2) = v2u{cvtpk(s[0], s[1]), cvtpk(s[2], s[3])};
      }
    } else if (LY == 0) {
      if ((ROLE == 0 && WOE > 0) || (ROLE == 1 && WFE > 0)) {
        while (*(volatile LAS u32*)(L + S_CNT + 4) < 4u) __builtin_amdgcn_s_sleep(1);
        asm volatile("" ::: "memory");
        load_wx(0, ROLE == 0 ? WOE : WFE);
      }
      const int tt = t - 256;
      SB();
      STAMP(30);
#pragma unroll
      for (int i = 0; i < 8; ++i) { const int idx = tt + 256 * i, row = idx >> 5, gr = idx & 31; *(LAS v4u*)(L + S_V + row * 512 + ((gr ^ (4 * (row & 3))) * 16)) = v_raw[i]; }
      STAMP(31);
      if (CODEPF) { u32 tmp_ = code_pf; pin(tmp_); }
    }
    if (LY == 1) BAR();
    STAMP(2 + SO);
    if (LO) {
      LAS unsigned char* Q = L + S_Q;
      f4 acc = {0.f, 0.f, 0.f, 0.f};
#pragma unroll
      for (int s = 0; s < 8; ++s) { const v4u qf = *(LAS v4u*)(Q + (n & 1) * 576 + s * 64 + u * 16); acc = mfma(kf[s], qf, acc); }
      if (n < 2) *(LAS f4*)(L + S_SC + n * 272 + (16 * w + 4 * u) * 4) = acc;
    }
    BAR();
    STAMP(3 + SO);
    if (LY == 0) {
#pragma unroll
      for (int tl = 0; tl < 2; ++tl)
#pragma unroll
        for (int st = 0; st < 2; ++st) {
          const int key = 32 * st + 8 * u + (n >> 2), gr = 4 * w + (n & 3);
          LAS unsigned char* a = L + S_V + key * 512 + ((gr ^ (4 * (key & 3))) * 16) + tl * 8;
          const v2u h0 = __builtin_bit_cast(v2u, __builtin_amdgcn_ds_read_tr16_b64_v4i16((LAS s4v*)a));
          const v2u h1 = __builtin_bit_cast(v2u, __builtin_amdgcn_ds_read_tr16_b64_v4i16((LAS s4v*)(a + 2048)));
          vf[tl][st] = v4u{h0.x, h0.y, h1.x, h1.y};
        }
    }
    {
      const float s0 = *(LAS float*)(L + S_SC + l * 4), s1 = *(LAS float*)(L + S_SC + 272 + l * 4);
      constexpr float CE = 0.0625f * 1.4426950408889634f;
      float m = row_max(sw32_max(s0, s1));
      m = sw16_max(m, m) * CE;
      auto ms = __builtin_amdgcn_permlane32_swap(__float_as_uint(m), __float_as_uint(m), false, false);
      const float p0 = __builtin_amdgcn_exp2f(fmaf(s0, CE, -__uint_as_float(ms[0]))), p1 = __builtin_amdgcn_exp2f(fmaf(s1, CE, -__uint_as_float(ms[1])));
      const u32 pk = cvtpk(p0, p1);
      *(LAS u16*)(L + S_P + w * 320 + l * 2) = (u16)pk;
      *(LAS u16*)(L + S_P + w * 320 + 160 + l * 2) = (u16)(pk >> 16);
      const v4u pf0 = *(LAS v4u*)(L + S_P + w * 320 + (n & 1) * 160 + u * 16), pf1 = *(LAS v4u*)(L + S_P + w * 320 + (n & 1) * 160 + 64 + u * 16);
      f4 o0 = {0.f, 0.f, 0.f, 0.f}, o1 = o0;
      o0 = mfma(vf[0][0], pf0, o0); o1 = mfma(vf[1][0], pf0, o1);
      o0 = mfma(vf[0][1], pf1, o0); o1 = mfma(vf[1][1], pf1, o1);
      stx4(v4u{cvtpk(o0[0], o0[1]), cvtpk(o0[2], o0[3]), cvtpk(o1[0], o1[1]), cvtpk(o1[2], o1[3])}, rws, o_cs, SC);
      if (LO) {
        float ls = row_sum(sw32_add(p0, p1)); ls = sw16_add(ls, ls);
        stx2(v2u{__float_as_uint(m), __float_as_uint(ls)}, rws, w == 0 ? o_ms : OOB, SM);
      }
    }
    STAMP(4 + SO);
    if (!(LY == 0 && !LO && ROLE != 0)) DLY(LY == 0 ? D_C0 : D_C1);
    if constexpr (ROLE == 0) {
      float Ls = 1.f;
      if (LY == 0) {
        if (LO) {
          if (w == 0) {
            v2u m0 = ldx2(rws, o_mg, SM);
            bool badm = max(m0.x, m0.y) == SENT;
            REPOLL(38, badm, m0 = ldx2(rws, o_mg, SM), max(m0.x, m0.y) == SENT);
            Ls = split_norm(split_weights(m0, L + S_E, l), m0);
          }
          STAMP(53);
        } else {
          load_wx(WOE, 8);
          SB();
#pragma unroll
          for (int i = 0; i < 8; ++i) *(LAS v4u*)(L + S_WX + (t - 256 + 256 * i) * 16) = wx_raw[i];
        }
        BAR();
        STAMP(54);
        DLY(D_C0B);
      }
      if (LY == 1) STAMP(48);
      v2u ml = {0u, 0u};
      if (LY == 1) ml = ldx2(rws, o_mg, SM);
      v4u a0 = ldx4(rws, o_cg, SC), a1 = ldx4(rws, o_cg + 1024, SC);
      if (LY == 0) {
        SB();
#pragma unroll
        for (int ks = 0; ks < 2; ++ks)
#pragma unroll
          for (int ct = 0; ct < 4; ++ct) {
            LAS unsigned char* a = L + S_WX + (64 * (w >> 1) + 32 * ks + 8 * u + (n >> 2)) * 128 + ct * 32 + (n & 3) * 8;
            const v2u h0 = __builtin_bit_cast(v2u, __builtin_amdgcn_ds_read_tr16_b64_v4i16((LAS s4v*)a));
            const v2u h1 = __builtin_bit_cast(v2u, __builtin_amdgcn_ds_read_tr16_b64_v4i16((LAS s4v*)(a + 512)));
            wx[4 * ks + ct] = v4u{h0.x, h0.y, h1.x, h1.y};
          }
      }
      const float xres = (g == 0 && hh == 0) ? bflo(*(LAS u16*)(L + S_XB + (64 * cb + l) * 2)) : 0.f;
      bool bad = max(max(vmax(a0), vmax(a1)), max(ml.x, ml.y)) == SENT;
      if (LY == 0) REPOLL(34, bad, (a0 = ldx4(rws, o_cg, SC), a1 = ldx4(rws, o_cg + 1024, SC)), max(vmax(a0), vmax(a1)) == SENT);
      else REPOLL(42, bad, (ml = ldx2(rws, o_mg, SM), a0 = ldx4(rws, o_cg, SC), a1 = ldx4(rws, o_cg + 1024, SC)), max(max(vmax(a0), vmax(a1)), max(ml.x, ml.y)) == SENT);
      STAMP(5 + SO);
      f4 d[4];
#pragma unroll
      for (int ct = 0; ct < 4; ++ct) d[ct] = mfma(a0, wx[ct], f4{0.f, 0.f, 0.f, 0.f});
#pragma unroll
      for (int ct = 0; ct < 4; ++ct) d[ct] = mfma(a1, wx[4 + ct], d[ct]);
      LAS unsigned char* E = L + S_E + (LY == 0 ? 0 : w * 128);
      float e1 = 0.f;
      if (LY == 1) e1 = split_weights(ml, E, l);
      const f4 ev = *(LAS f4*)(E + (16 * (w & 1) + 4 * u) * 4);
      if (LY == 1 && LO) Ls = split_norm(e1, ml);
      float cs[4];
#pragma unroll
      for (int ct = 0; ct < 4; ++ct) cs[ct] = ev[0] * d[ct][0] + ev[1] * d[ct][1] + ev[2] * d[ct][2] + ev[3] * d[ct][3];
      const float z = sw16_add(sw32_add(cs[0], cs[2]), sw32_add(cs[1], cs[3]));
      *(LAS float*)(L + S_PART + w * 256 + l * 4) = z;
      BAR();
      if (LO && w == 0) {
        float s = 0.f;
#pragma unroll
        for (int i = 0; i < 8; ++i) s += *(LAS float*)(L + S_PART + i * 256 + l * 4);
        s = fmaf(s, __builtin_amdgcn_rcpf(Ls), xres);
        const u32 pk = cvtpk(s, dpp<QX1>(s));
#pragma unroll
        for (int r = 0; r < 8; ++r) { if (LY == 0) stx1(pk, rws, o_pub0, r * WD0_REP); else stx1(pk, rws, o_pub1, srep[r]); }
      }
      STAMP(6 + SO);
      if (LY == 1) return;
      if (LO) vote();
      if (LO && w == 1) {
        DLY(D_DSH);
        const u32 o = l < 16 ? W_D0 + g * WD0_REP + l * 896 + 6 * 128 : OOB;
        u32 v = ldx1(rws, o, 0);
        bool badv = v == SENT;
        REPOLL(39, badv, v = ldx1(rws, o, 0), v == SENT);
        if (l == 0) *(LAS u64*)(L + S_TML) = __builtin_amdgcn_s_memrealtime();
      }
      BAR();
      if (!LO) set_sides();
      wait_until(*(LAS u64*)(L + S_TML) + DLV(D_A1));
    } else {
      u64 t_ml = 0;
      if (LY == 0 && LO) vote();
      if (LO && w == 0) {
        v2u ml = ldx2(rws, o_mg, SM);
        bool bad = max(ml.x, ml.y) == SENT;
        REPOLL(34 + SO / 2, bad, ml = ldx2(rws, o_mg, SM), max(ml.x, ml.y) == SENT);
        t_ml = __builtin_amdgcn_s_memrealtime();
        if (l == 0) *(LAS u64*)(L + S_TML) = t_ml;
      }
      if (LY == 1) { BAR(); t_ml = *(LAS u64*)(L + S_TML); }
      STAMP(5 + SO);
      if (LY == 0) {
        if (LO) asm volatile("s_waitcnt vmcnt(0)" ::: "memory");
        if (ROLE == 2) {
          const rsrc_t rw2 = mkrs(p.w2, DM * FF * 2);
          const u32 o = (w < 7 && 128 * wa + 2 * l < DM) ? (224 * g + 32 * w) * (DM * 2) + wa * 256 + l * 4 : OOB;
#pragma unroll
          for (int i = 0; i < 32; ++i) w2_raw[i] = __builtin_amdgcn_raw_buffer_load_b32(rw2, o + i * (DM * 2), 0, NTW);
          SB();
          BAR();
        } else {
          if (!LO) {
            const int tt = t - 256;
            load_wx(WFE, 7);
            SB();
#pragma unroll
            for (int i = 0; i < 7; ++i) { const int idx = tt + 256 * i; *(LAS v4u*)(L + S_WX + ((idx ^ ((idx >> 5) & 7)) * 16)) = wx_raw[i]; }
          }
          BAR();
          v2u d[8];
#pragma unroll
          for (int jj = 0; jj < 8; ++jj) d[jj] = *(LAS v2u*)(L + S_WX + (((32 * lc + 4 * jj + (w >> 1)) ^ (lc & 7)) * 16) + (w & 1) * 8);
#pragma unroll
          for (int q = 0; q < 2; ++q)
#pragma unroll
            for (int i = 0; i < 4; ++i) { wx[2 * q][i] = __builtin_amdgcn_perm(d[2 * i + 1][q], d[2 * i][q], 0x05040100u); wx[2 * q + 1][i] = __builtin_amdgcn_perm(d[2 * i + 1][q], d[2 * i][q], 0x07060302u); }
          S13 = rows4(dot8(wx[0], onem4), dot8(wx[1], onem4), dot8(wx[2], onem4), dot8(wx[3], onem4));
        }
        t_ml = *(LAS u64*)(L + S_TML);
        if (!LO) set_sides();
      }
      wait_until(t_ml + DLV(LY == 0 ? D_DS0 : D_DS1));
      if (LY == 1) STAMP(49);
      v4u dv[4];
      auto dso = [&](int i) { return LY == 0 ? (u32)(i * 4 * 896) : sg + (i & 1) * 4096 + (i >> 1) * 16384; };
      if (LO) {
#pragma unroll
        for (int i = 0; i < 4; ++i) dv[i] = ldx4(rws, o_dg, dso(i));
      }
      if (LY == 0 && ROLE == 2) {
#pragma unroll
        for (int i = 0; i < 16; ++i) { wx[i >> 2][i & 3] = __builtin_amdgcn_perm(w2_raw[2 * i + 1], w2_raw[2 * i], 0x05040100u);
                                       wx[4 + (i >> 2)][i & 3] = __builtin_amdgcn_perm(w2_raw[2 * i + 1], w2_raw[2 * i], 0x07060302u); }
      }
      if (LO) {
        auto dbad = [&]() { return max(max(vmax(dv[0]), vmax(dv[1])), max(vmax(dv[2]), vmax(dv[3]))) == SENT; };
        bool bad = dbad();
        REPOLL(35 + SO / 2, bad, (dv[0] = ldx4(rws, o_dg, dso(0)), dv[1] = ldx4(rws, o_dg, dso(1)), dv[2] = ldx4(rws, o_dg, dso(2)), dv[3] = ldx4(rws, o_dg, dso(3))), dbad());
        STAMP(7 + SO);
        f4 x1 = {0.f, 0.f, 0.f, 0.f};
#pragma unroll
        for (int i = 0; i < 4; ++i) x1 = mfma(selm, dv[i], x1);
        if (a_xw < DM) {
          if (ROLE == 1) *(LAS v2u*)(L + S_XB + a_xw * 2) = v2u{cvtpk(x1[0], x1[1]), cvtpk(x1[2], x1[3])};
          else *(LAS f4*)(L + S_X1 + a_xw * 4) = x1;
        }
      }
      if constexpr (ROLE == 1) {
        BAR();
        const v4u xb = *(LAS v4u*)(L + S_XB + l * 16);
        float mu, r; ln_stats(xb, mu, r);
        float y = (rows4(dot8(xb, wx[0]), dot8(xb, wx[1]), dot8(xb, wx[2]), dot8(xb, wx[3])) - mu * S13) * r;
        if (!isup) y *= __builtin_amdgcn_rcpf(1.f + __builtin_amdgcn_exp2f(-1.4426950408889634f * y));
        auto s = __builtin_amdgcn_permlane16_swap(__float_as_uint(y), __float_as_uint(y), false, false);
        const u32 pk = cvtpk(__uint_as_float(s[0]), __uint_as_float(s[1]));
        auto s2 = __builtin_amdgcn_permlane32_swap(pk, pk, false, false);
        stx2(v2u{s2[0], s2[1]}, rws, o_pub0, SH + sg);
        STAMP(8 + SO);
        if (LY == 1) return;
        DLY(D_A2);
      } else {
        BAR();
        u32 sfin0 = 0, sfin1 = 0;
        if (LY == 1) {
#pragma unroll
          for (int r = 0; r < 4; ++r) if (r == wa) { sfin0 = srep[2 * r]; sfin1 = srep[2 * r + 1]; }
        }
        DLY(LY == 0 ? D_H0 : D_H1);
        u32 hg = ldx1(rws, o_gat, SH + sg), hu = ldx1(rws, o_gat, SH + sg + LH_UP);
        bool bad2 = max(hg, hu) == SENT;
        REPOLL(36 + SO / 2, bad2, (hg = ldx1(rws, o_gat, SH + sg), hu = ldx1(rws, o_gat, SH + sg + LH_UP)), max(hg, hu) == SENT);
        STAMP(8 + SO);
        const u32 hp = cvtpk(bflo(hg) * bflo(hu), bfhi(hg) * bfhi(hu));
        f2 acc = {0.f, 0.f};
        static_for<16>([&](auto I) { constexpr int i = decltype(I)::value;
          const u32 hb = (u32)__builtin_amdgcn_mov_dpp((int)hp, 0x150 + i, 0xf, 0xf, true);
          acc.x = dot2(hb, wx[i >> 2][i & 3], acc.x); acc.y = dot2(hb, wx[4 + (i >> 2)][i & 3], acc.y); });
        *(LAS f2*)(L + S_PART + w * 512 + l * 8) = acc;
        BAR();
        if (LO && w == 0) {
          f2 s = {0.f, 0.f};
          if (g == 0) s = *(LAS f2*)(L + S_X1 + min(128 * wa + 2 * l, DM - 2) * 4);
#pragma unroll
          for (int i = 0; i < 7; ++i) s += *(LAS f2*)(L + S_PART + i * 512 + l * 8);
          const u32 pk = cvtpk(s.x, s.y);
          if (LY == 0) {
#pragma unroll
            for (int r = 0; r < 8; ++r) stx1(pk, rws, o_pub0, srep[r]);
          } else {
            stx1(pk, rws, o_pub0, SA + sfin0); stx1(pk, rws, o_pub0, SA + sfin1);
          }
        }
        STAMP(9 + SO);
        if (LY == 1) {
          if (LO && w == 0 && j == 28 && g < 7) {
            DLY(D_F);
            const u32 o = n < 8 ? two(L_A + g * LA_REP + u * 1024 + g * 128 + n * 16) : OOB;
            v4u f0 = ldx4(rws, o, SA + sg), f1 = ldx4(rws, o, SA + sg + 4096);
            bool bad3 = max(vmax(f0), vmax(f1)) == SENT;
            REPOLL(45, bad3, (f0 = ldx4(rws, o, SA + sg), f1 = ldx4(rws, o, SA + sg + 4096)), max(vmax(f0), vmax(f1)) == SENT);
            const f4 y = mfma(selm, f1, mfma(selm, f0, f4{0.f, 0.f, 0.f, 0.f}));
            if (u < 2 && n < 8) *(v2u*)(p.out + 64 * g + 8 * n + 4 * u) = v2u{cvtpk(y[0], y[1]), cvtpk(y[2], y[3])};
            if (g == 0 && l == 0) stx1(T0, rws, WS_EP, 0);
            STAMP(10 + SO);
          }
          return;
        }
        DLY(D_A3);
      }
    }
    {
      if (LO) {
        v4u a0 = ldx4(rws, o_ag, sg), a1 = ldx4(rws, o_ag, sg + 4096);
        bool bad = max(vmax(a0), vmax(a1)) == SENT;
        REPOLL(37, bad, (a0 = ldx4(rws, o_ag, sg), a1 = ldx4(rws, o_ag, sg + 4096)), max(vmax(a0), vmax(a1)) == SENT);
        STAMP(10 + SO);
        const f4 x2 = mfma(selm, a1, mfma(selm, a0, f4{0.f, 0.f, 0.f, 0.f}));
        if (a_xw < DM) *(LAS v2u*)(L + S_XB + a_xw * 2) = v2u{cvtpk(x2[0], x2[1]), cvtpk(x2[2], x2[3])};
      }
      BAR();
      if (LO) {
        v4u xb = *(LAS v4u*)(L + S_XB + l * 16);
        const v2u qv = q_phase(xb);
        stx2(qv, rws, o_qs1, sg);
        STAMP(17);
        DLY(D_B1);
      }
    }
  };
  layer(std::integral_constant<int, 0>{});
  layer(std::integral_constant<int, 1>{});
  STAMP_FLUSH();
  asm volatile("; end of program %0 %1" :: "n"(ROLE), "n"(LO ? 1 : 0));
}

__global__ void __launch_bounds__(NT, 2) mega(Params p) {
  extern __shared__ __attribute__((aligned(16))) unsigned char lds_[];
  LAS unsigned char* L = (LAS unsigned char*)lds_;
  const int c = blockIdx.x, t = threadIdx.x, w = __builtin_amdgcn_readfirstlane(t >> 6), j = c >> 3;
  u64 code; u32 len;
  asm volatile("s_getpc_b64 %0\n.Lmk_pc:\n\ts_mov_b32 %1, " FUNC_END "-.Lmk_pc" : "=s"(code), "=s"(len));
  Pre pre;
  STAMP(0);
  pre.t_start = __builtin_amdgcn_s_memrealtime();
  if (w >= 4) prologue1<false>(p, L, c, t, w, code, len, pre); else prologue1<true>(p, L, c, t, w, code, len, pre);
  SB();
  __builtin_amdgcn_s_barrier();
  SB();
  STAMP(12);
  prologue2(p, c, t, w < 4, pre);
  SB();
  int w2 = w; asm volatile("" : "+s"(w2));
  if (w2 >= 4) {
    if (j < 14) program<0, false>(p, L, c, t, w, pre); else if (j < 28) program<1, false>(p, L, c, t, w, pre); else program<2, false>(p, L, c, t, w, pre);
  } else {
    if (j < 14) program<0, true>(p, L, c, t, w, pre); else if (j < 28) program<1, true>(p, L, c, t, w, pre); else program<2, true>(p, L, c, t, w, pre);
  }
}
__device__ __attribute__((noinline, used)) void code_end() { asm volatile("mk_code_end:"); }
}

extern "C" void kernel_launch(void* const* d_in, const int* in_sizes, int n_in, void* d_out, int out_size, void* d_ws, size_t ws_size, hipStream_t stream) {
  mk::Params p;
  p.x = (const mk::u16*)d_in[0]; p.k = (const mk::u16*)d_in[1]; p.v = (const mk::u16*)d_in[2];
  p.wq = (const mk::u16*)d_in[3]; p.wo = (const mk::u16*)d_in[4];
  p.w1 = (const mk::u16*)d_in[6]; p.w3 = (const mk::u16*)d_in[7]; p.w2 = (const mk::u16*)d_in[8];
  p.out = (mk::u16*)d_out; p.ws = (unsigned char*)d_ws;
  (void)hipFuncSetAttribute((const void*)mk::mega, hipFuncAttributeMaxDynamicSharedMemorySize, mk::LDS_BYTES);
  hipLaunchKernelGGL(mk::mega, dim3(256), dim3(mk::NT), mk::LDS_BYTES, stream, p);
}
```
